```python
import torch

def _softmax(x, axis=-1):
    return torch.softmax(x, dim=axis)
def _gelu(x):
    return torch.nn.functional.gelu(x, approximate="tanh")
def _silu(x):
    return torch.nn.functional.silu(x)
def _sigmoid(x):
    return torch.sigmoid(x)
def _layernorm(x, axis=-1, eps=1e-5):
    m = x.mean(dim=axis, keepdim=True)
    v = x.var(dim=axis, keepdim=True, unbiased=False)
    return (x - m) / torch.sqrt(v + eps)
def _scatter_add(tgt, idx, src, axis=0):
    return tgt.clone().index_add_(axis, idx, src)
def _segment_sum(src, seg, n):
    out = torch.zeros((n,) + src.shape[1:], dtype=src.dtype)
    return out.index_add_(0, seg, src)
def _pad(x, pw):
    flat = tuple(v for pair in reversed(pw) for v in pair)
    return torch.nn.functional.pad(x, flat)

kernel_name = 'recurrent_scan__mha_block_cached__dense_activation_squared_relu__decode'

def setup_inputs():

    _w = (lambda a: (lambda t: t.to(torch.bfloat16) if t.is_floating_point() else t)(torch.as_tensor(a)))
    def _A_setup():
        B, Sq, D = 2, 1, 576
        H, Sk, Dh = 32, 4096, 128
        return {
            "x": _w((lambda *s: torch.randn(*s, dtype=torch.bfloat16) * 0.05)(B, Sq, D)),
            "k_cache": _w((lambda *s: torch.randn(*s, dtype=torch.bfloat16) * 0.8)(B, H, Sk, Dh)),
            "v_cache": _w((lambda *s: torch.randn(*s, dtype=torch.bfloat16) * 0.2)(B, H, Sk, Dh)),
            "wq": _w((lambda *s: torch.randn(*s, dtype=torch.bfloat16) * 0.8)(D, H * Dh) * (D ** -0.5)),
            "wo": _w((lambda *s: torch.randn(*s, dtype=torch.bfloat16) * 0.2)(H * Dh, D) * ((H * Dh) ** -0.5)),
        }
    _A_in = _A_setup()

    def _B_setup():
        B, S, D, F = 2, 1, 576, 4608
        return {
            "x": _w((lambda *s: torch.randn(*s, dtype=torch.bfloat16) * 0.05)(B, S, D)),
            "w1": _w((lambda *s: torch.randn(*s, dtype=torch.bfloat16) * 0.1)(D, F)),
            "b1": _w((lambda *s: torch.randn(*s, dtype=torch.bfloat16) * 0.8)(F)),
            "w2": _w((lambda *s: torch.randn(*s, dtype=torch.bfloat16) * 0.1)(F, D)),
            "b2": _w((lambda *s: torch.randn(*s, dtype=torch.bfloat16) * 12.8)(D)),
        }
    _B_in = _B_setup()

    return {
        "A_x": _A_in["x"],
        "A_k_cache": _A_in["k_cache"],
        "A_v_cache": _A_in["v_cache"],
        "A_wq": _A_in["wq"],
        "A_wo": _A_in["wo"],
        "B_x": _B_in["x"],
        "B_w1": _B_in["w1"],
        "B_b1": _B_in["b1"],
        "B_w2": _B_in["w2"],
        "B_b2": _B_in["b2"],
    }

def reference(A_x, A_k_cache, A_v_cache, A_wq, A_wo, B_x, B_w1, B_b1, B_w2, B_b2):
    def _A_ref(x, k_cache, v_cache, wq, wo):
        B, Sq, D = x.shape
        H, Dh = 32, 128
        q = (lambda x, *d: x.permute(*d))((x @ wq).reshape(B, Sq, H, Dh), 0, 2, 1, 3)
        scores = torch.einsum("bhqd,bhkd->bhqk", q, k_cache) * (Dh ** -0.5)
        p = _softmax(scores, axis=-1)
        o = torch.einsum("bhqk,bhkd->bhqd", p, v_cache)
        return (lambda x, *d: x.permute(*d))(o, 0, 2, 1, 3).reshape(B, Sq, H * Dh) @ wo

    def _B_ref(x, w1, b1, w2, b2):
        h = torch.einsum("bsd,df->bsf", x, w1) + b1
        zero = torch.zeros_like(h)
        r = torch.where(h > zero, h, zero)
        h = r * r
        out = torch.einsum("bsf,fd->bsd", h, w2) + b2
        return out

    x = A_x
    xn = _layernorm(x, axis=-1)
    s = _A_ref(xn, A_k_cache, A_v_cache, A_wq, A_wo)
    g = _silu(_B_ref(xn, B_w1, B_b1, B_w2, B_b2))
    return x + s * g
```

```cpp
#include <hip/hip_runtime.h>
#include <hip/hip_bf16.h>
#include <stdint.h>
#include <type_traits>

#define GAS __attribute__((address_space(1)))
typedef unsigned v4u __attribute__((ext_vector_type(4)));
typedef __bf16 bf16x2 __attribute__((ext_vector_type(2)));
typedef unsigned short u16;

constexpr int B = 2, D = 576, H = 32, SK = 4096, DH = 128, F = 4608, HD = H * DH;
constexpr int NSPLIT = 4;

__device__ __forceinline__ float bf2f(u16 b) { return __builtin_bit_cast(float, (unsigned)b << 16); }
__device__ __forceinline__ float rbf(float f) {
  return __bfloat162float(__float2bfloat16(f));
}
__device__ __forceinline__ u16 f2bf(float f) { __hip_bfloat16 h = __float2bfloat16(f); return __builtin_bit_cast(u16, h); }
__device__ __forceinline__ float dot2(unsigned k, unsigned q, float acc) {
  return __builtin_amdgcn_fdot2_f32_bf16(__builtin_bit_cast(bf16x2, k), __builtin_bit_cast(bf16x2, q), acc, false);
}
template <int CTRL> __device__ __forceinline__ float dpp(float x) {
  return __builtin_bit_cast(float, __builtin_amdgcn_mov_dpp(__builtin_bit_cast(int, x), CTRL, 0xf, 0xf, true));
}
constexpr int XOR1 = 0xB1, XOR2 = 0x4E, XOR7 = 0x141, XOR8 = 0x128, ROR4 = 0x124;
__device__ __forceinline__ float xrow16_max(float x) {
  auto s = __builtin_amdgcn_permlane16_swap(__float_as_uint(x), __float_as_uint(x), false, false);
  x = fmaxf(__uint_as_float(s[0]), __uint_as_float(s[1]));
  auto t = __builtin_amdgcn_permlane32_swap(__float_as_uint(x), __float_as_uint(x), false, false);
  return fmaxf(__uint_as_float(t[0]), __uint_as_float(t[1]));
}
__device__ __forceinline__ float xrow16_sum(float x) {
  auto s = __builtin_amdgcn_permlane16_swap(__float_as_uint(x), __float_as_uint(x), false, false);
  x = __uint_as_float(s[0]) + __uint_as_float(s[1]);
  auto t = __builtin_amdgcn_permlane32_swap(__float_as_uint(x), __float_as_uint(x), false, false);
  return __uint_as_float(t[0]) + __uint_as_float(t[1]);
}

template <int I> __device__ __forceinline__ float rowbcast(float x) {
  return __builtin_bit_cast(float, __builtin_amdgcn_update_dpp(0, __builtin_bit_cast(int, x), 0x150 + I, 0xf, 0xf, true));
}
template <int I, int N, class Fn> __device__ __forceinline__ void static_for(Fn&& f) {
  if constexpr (I < N) { f(std::integral_constant<int, I>{}); static_for<I + 1, N>(f); }
}
__device__ __forceinline__ void layernorm(const u16* x, float* xn  , float* red  ) {
  const int tid = threadIdx.x, nthr = blockDim.x;
  const int w = tid >> 6, lane = tid & 63;
  if (w < 2) {
    float v[9]; float s = 0.f;
    for (int i = 0; i < 9; ++i) { v[i] = bf2f(x[w * D + lane + 64 * i]); s += v[i]; }
    for (int m = 32; m >= 1; m >>= 1) s += __shfl_xor(s, m);
    const float mean = s * (1.0f / D);
    float ss = 0.f;
    for (int i = 0; i < 9; ++i) { const float d = v[i] - mean; ss += d * d; }
    for (int m = 32; m >= 1; m >>= 1) ss += __shfl_xor(ss, m);
    const float var = rbf(ss * (1.0f / D));
    const float mb = rbf(mean);
    const float den = rbf(sqrtf(rbf(var + 1e-5f)));
    for (int i = 0; i < 9; ++i) xn[w * D + lane + 64 * i] = rbf(rbf(v[i] - mb) / den);
  }
  __syncthreads();
}

__global__ void __launch_bounds__(256) k_prep(const u16* x, const u16* wq, const u16* w1, const u16* b1, u16* q, float* h2) {
  __shared__ float xn[2 * D];
  __shared__ float red[4 * 2 * 64];
  layernorm(x, xn, red);
  const int blk = blockIdx.x;
  const bool isq = blk < 64;
  const int col0 = isq ? blk * 64 : (blk - 64) * 64;
  const u16* W = isq ? wq : w1;
  const int ld = isq ? HD : F;
  const int w = threadIdx.x >> 6, lane = threadIdx.x & 63;
  float a0 = 0.f, a1 = 0.f;
  for (int r = w; r < D; r += 4) {
    const float wv = bf2f(W[(size_t)r * ld + col0 + lane]);
    a0 = fmaf(xn[r], wv, a0); a1 = fmaf(xn[D + r], wv, a1);
  }
  red[(w * 2 + 0) * 64 + lane] = a0; red[(w * 2 + 1) * 64 + lane] = a1;
  __syncthreads();
  if (threadIdx.x < 128) {
    const int b = threadIdx.x >> 6;
    float s = 0.f;
    for (int ww = 0; ww < 4; ++ww) s += red[(ww * 2 + b) * 64 + lane];
    if (isq) q[b * HD + col0 + lane] = f2bf(s);
    else { float hh = rbf(rbf(s) + bf2f(b1[col0 + lane])); hh = hh > 0.f ? hh : 0.f; h2[b * F + col0 + lane] = rbf(hh * hh); }
  }
}

__global__ void __launch_bounds__(512) k_attn(const u16* kc, const u16* vc, const u16* q, float* po, float* pml) {
  __shared__ float mrg[8 * (DH + 4)];
  const int blk = blockIdx.x;
  const int bh = blk >> 2, s = blk & 3;
  const int b = bh >> 5, h = bh & 31;
  const int tid = threadIdx.x, lane = tid & 63, w = __builtin_amdgcn_readfirstlane(tid >> 6), r = lane >> 4, c = lane & 15;
  const size_t base = ((size_t)bh * SK + s * 1024 + w * 128) * DH;
  const GAS v4u* kp = (const GAS v4u*)(kc + base) + lane;
  const GAS v4u* vp = (const GAS v4u*)(vc + base) + lane;
  v4u buf[32];
#pragma unroll
  for (int i = 0; i < 32; ++i) buf[i] = __builtin_nontemporal_load(kp + i * 64);
  __builtin_amdgcn_sched_barrier(0);
  const v4u qv = *(const GAS v4u*)(q + b * HD + h * DH + 8 * c);
  float sc[2];
#pragma unroll
  for (int half = 0; half < 2; ++half) {
    float x[16];
#pragma unroll
    for (int i = 0; i < 16; ++i) {
      const v4u kk = buf[half * 16 + i];
      float d = dot2(kk.x, qv.x, 0.f); d = dot2(kk.y, qv.y, d); d = dot2(kk.z, qv.z, d); d = dot2(kk.w, qv.w, d);
      x[i] = d;
      buf[half * 16 + i] = __builtin_nontemporal_load(vp + (half * 16 + i) * 64);
      __builtin_amdgcn_sched_barrier(0);
    }
#define TR_STEP(HALF, CTRL, BIT) _Pragma("unroll") for (int hh = 0; hh < (HALF); ++hh) { \
      const float lo_ = x[hh], hi_ = x[hh + (HALF)]; const float t1_ = lo_ + dpp<CTRL>(lo_), t2_ = hi_ + dpp<CTRL>(hi_); \
      x[hh] = (c & (BIT)) ? t2_ : t1_; }
    TR_STEP(8, XOR8, 8) TR_STEP(4, XOR7, 4) TR_STEP(2, XOR2, 2) TR_STEP(1, XOR1, 1)
#undef TR_STEP
    sc[half] = rbf(rbf(x[0]) * 0.08838834764831845f);
  }
  float m = fmaxf(sc[0], sc[1]);
  m = fmaxf(m, dpp<XOR1>(m)); m = fmaxf(m, dpp<XOR2>(m)); m = fmaxf(m, dpp<XOR7>(m)); m = fmaxf(m, dpp<XOR8>(m)); m = xrow16_max(m);
  float p[2];
  p[0] = __builtin_amdgcn_exp2f((sc[0] - m) * 1.4426950408889634f);
  p[1] = __builtin_amdgcn_exp2f((sc[1] - m) * 1.4426950408889634f);
  float l = p[0] + p[1];
  l += dpp<XOR1>(l); l += dpp<XOR2>(l); l += dpp<XOR7>(l); l += dpp<XOR8>(l); l = xrow16_sum(l);
  float acc[8];
#pragma unroll
  for (int k = 0; k < 8; ++k) acc[k] = 0.f;
  static_for<0, 32>([&](auto ic) {
    constexpr int i = decltype(ic)::value;
    const float pv = rowbcast<(i & 15)>(p[i >> 4]);
    const v4u vv = buf[i];
#pragma unroll
    for (int k = 0; k < 4; ++k) {
      acc[2 * k] = fmaf(pv, __builtin_bit_cast(float, vv[k] << 16), acc[2 * k]);
      acc[2 * k + 1] = fmaf(pv, __builtin_bit_cast(float, vv[k] & 0xffff0000u), acc[2 * k + 1]);
    }
  });
#pragma unroll
  for (int k = 0; k < 8; ++k) acc[k] = xrow16_sum(acc[k]);
  float* rec = mrg + w * (DH + 4);
  if (r == 0) {
#pragma unroll
    for (int k = 0; k < 8; ++k) rec[4 + 8 * c + k] = acc[k];
    if (c == 0) { rec[0] = m; rec[1] = l; }
  }
  __syncthreads();
  if (tid < DH) {
    float M = -INFINITY;
    for (int ww = 0; ww < 8; ++ww) M = fmaxf(M, mrg[ww * (DH + 4)]);
    float L = 0.f, o = 0.f;
    for (int ww = 0; ww < 8; ++ww) { const float* rr = mrg + ww * (DH + 4); const float wt = __builtin_amdgcn_exp2f((rr[0] - M) * 1.4426950408889634f); L = fmaf(wt, rr[1], L); o = fmaf(wt, rr[4 + tid], o); }
    po[(size_t)blk * DH + tid] = o;
    if (tid == 0) { pml[blk * 2] = M; pml[blk * 2 + 1] = L; }
  }
}

__global__ void __launch_bounds__(1024) k_final(const u16* x, const u16* wo, const u16* w2, const u16* b2, const float* po, const float* pml, const float* h2, u16* out) {
  __shared__ float o[HD];
  __shared__ float red[2][64][16];
  const int b = blockIdx.x / 36, cg = blockIdx.x % 36;
  const int tid = threadIdx.x;
  for (int i = tid; i < HD; i += 1024) {
    const int h = i >> 7, d = i & 127;
    const int base = (b * H + h) * NSPLIT;
    float M = -INFINITY;
    for (int s = 0; s < NSPLIT; ++s) M = fmaxf(M, pml[(base + s) * 2]);
    float L = 0.f, a = 0.f;
    for (int s = 0; s < NSPLIT; ++s) { const float wt = __builtin_amdgcn_exp2f((pml[(base + s) * 2] - M) * 1.4426950408889634f); L = fmaf(wt, pml[(base + s) * 2 + 1], L); a = fmaf(wt, po[(size_t)(base + s) * DH + d], a); }
    o[i] = rbf(a / L);
  }
  __syncthreads();
  const int col = cg * 16 + (tid & 15), rg = tid >> 4;
  float a = 0.f, f = 0.f;
  for (int r = rg; r < HD; r += 64) a = fmaf(o[r], bf2f(wo[(size_t)r * D + col]), a);
  for (int r = rg; r < F; r += 64) f = fmaf(h2[b * F + r], bf2f(w2[(size_t)r * D + col]), f);
  red[0][rg][tid & 15] = a; red[1][rg][tid & 15] = f;
  __syncthreads();
  if (tid < 16) {
    float s = 0.f, ff = 0.f;
    for (int i = 0; i < 64; ++i) { s += red[0][i][tid]; ff += red[1][i][tid]; }
    s = rbf(s);
    ff = rbf(rbf(ff) + bf2f(b2[col]));
    const float g = rbf(ff / (1.0f + __expf(-ff)));
    out[b * D + col] = f2bf(bf2f(x[b * D + col]) + rbf(s * g));
  }
}

extern "C" void kernel_launch(void* const* d_in, const int* in_sizes, int n_in, void* d_out, int out_size, void* d_ws, size_t ws_size, hipStream_t stream) {
  const u16* x = (const u16*)d_in[0];
  const u16* kc = (const u16*)d_in[1];
  const u16* vc = (const u16*)d_in[2];
  const u16* wq = (const u16*)d_in[3];
  const u16* wo = (const u16*)d_in[4];
  const u16* w1 = (const u16*)d_in[6];
  const u16* b1 = (const u16*)d_in[7];
  const u16* w2 = (const u16*)d_in[8];
  const u16* b2 = (const u16*)d_in[9];
  char* ws = (char*)d_ws;
  u16* q = (u16*)ws;
  float* h2 = (float*)(ws + 65536);
  float* po = (float*)(ws + 131072);
  float* pml = (float*)(ws + 131072 + 262144);
  k_prep<<<136, 256, 0, stream>>>(x, wq, w1, b1, q, h2);
  k_attn<<<256, 512, 0, stream>>>(kc, vc, q, po, pml);
  k_final<<<72, 1024, 0, stream>>>(x, wo, w2, b2, po, pml, h2, (u16*)d_out);
}
```

```cpp
#include <hip/hip_runtime.h>
#include <hip/hip_bf16.h>
#include <stdint.h>
#include <type_traits>

namespace {

#define GAS __attribute__((address_space(1)))
#define LAS __attribute__((address_space(3)))
typedef unsigned v4u __attribute__((ext_vector_type(4)));
typedef float f32x4 __attribute__((ext_vector_type(4)));
typedef __bf16 bf16x2 __attribute__((ext_vector_type(2)));
typedef __bf16 bf16x8 __attribute__((ext_vector_type(8)));
typedef unsigned long long u64;
typedef unsigned short u16;

constexpr int D = 576, H = 32, SK = 4096, DH = 128, F = 4608, HD = H * DH;
constexpr int NWG = 256, NTHR = 320;

#define NSLOT 9
#define KV_NT 1
constexpr int SLOT = 16384;
constexpr int NFILL = 32;
constexpr int MISC = NSLOT * SLOT;
constexpr int TILE_A = MISC;
constexpr int TILE_B = MISC + 4096;
constexpr int QB = MISC + 12288;
constexpr int X2H = QB + 256;
constexpr int X2O = X2H + 576;
constexpr int FLAGS = X2O + 512;
constexpr int LDS_BYTES = FLAGS + 128;
static_assert(LDS_BYTES <= 163840, "LDS");
static_assert((FLAGS & 15) == 0, "align");
constexpr int WSLOT = NFILL % NSLOT;
static_assert(WSLOT + 2 < NSLOT, "w region must not wrap");
constexpr int W2_OFF = WSLOT * SLOT, WO_OFF = W2_OFF + 144 * 144;
constexpr unsigned W_NEED_FREE = NFILL + 3 - NSLOT;
enum { F_FULL = 0, F_CQ = 1, F_CH = 2, F_CQR = 3, F_FREE = 4  , F_MW = 8  , F_LW = 12  , F_CM = 16, F_CO = 17, F_CF = 18 };

constexpr size_t WS_E = 0;
constexpr size_t WS_QP = 4096;
constexpr size_t WS_HP = WS_QP + (size_t)NWG * 256 * 8;
constexpr size_t WS_OP = WS_HP + (size_t)NWG * 512 * 8;
constexpr size_t WS_FIN = WS_OP + (size_t)NWG * 136 * 8;
constexpr size_t WS_END = WS_FIN + (size_t)8 * 18 * 32 * 16 * 8;
constexpr size_t WS_STAMP = 8u << 20;
constexpr unsigned SPIN_CAP = 1u << 15;

struct Params {
  const u16* x; const u16* kc; const u16* vc; const u16* wq; const u16* wo;
  const u16* w1; const u16* b1; const u16* w2; const u16* b2; u16* out; unsigned char* ws;
};
static_assert(sizeof(Params) == 88, "no padding");

__device__ __forceinline__ float bf2f(unsigned b) { return __builtin_bit_cast(float, b << 16); }
__device__ __forceinline__ unsigned f2bf(float f) { return (unsigned)__builtin_bit_cast(u16, __float2bfloat16(f)); }
__device__ __forceinline__ float rbf(float f) { return bf2f(f2bf(f)); }
__device__ __forceinline__ float dot2(unsigned k, unsigned q, float acc) {
  return __builtin_amdgcn_fdot2_f32_bf16(__builtin_bit_cast(bf16x2, k), __builtin_bit_cast(bf16x2, q), acc, false);
}
template <int CTRL> __device__ __forceinline__ float dpp(float x) {
  return __builtin_bit_cast(float, __builtin_amdgcn_mov_dpp(__builtin_bit_cast(int, x), CTRL, 0xf, 0xf, true));
}
template <int I> __device__ __forceinline__ unsigned rowbcast(unsigned x) {
  return (unsigned)__builtin_amdgcn_update_dpp(0, (int)x, 0x150 + I, 0xf, 0xf, true);
}
constexpr int XOR1 = 0xB1, XOR2 = 0x4E, XOR7 = 0x141, XOR8 = 0x128;
__device__ __forceinline__ float sum16(float x) { x += dpp<XOR1>(x); x += dpp<XOR2>(x); x += dpp<XOR7>(x); x += dpp<XOR8>(x); return x; }
__device__ __forceinline__ float max16(float x) { x = fmaxf(x, dpp<XOR1>(x)); x = fmaxf(x, dpp<XOR2>(x)); x = fmaxf(x, dpp<XOR7>(x)); x = fmaxf(x, dpp<XOR8>(x)); return x; }
__device__ __forceinline__ float x16_sum(float x) { auto s = __builtin_amdgcn_permlane16_swap(__float_as_uint(x), __float_as_uint(x), false, false); return __uint_as_float(s[0]) + __uint_as_float(s[1]); }
__device__ __forceinline__ float x32_sum(float x) { auto s = __builtin_amdgcn_permlane32_swap(__float_as_uint(x), __float_as_uint(x), false, false); return __uint_as_float(s[0]) + __uint_as_float(s[1]); }
__device__ __forceinline__ float x16_max(float x) { auto s = __builtin_amdgcn_permlane16_swap(__float_as_uint(x), __float_as_uint(x), false, false); return fmaxf(__uint_as_float(s[0]), __uint_as_float(s[1])); }
__device__ __forceinline__ float x32_max(float x) { auto s = __builtin_amdgcn_permlane32_swap(__float_as_uint(x), __float_as_uint(x), false, false); return fmaxf(__uint_as_float(s[0]), __uint_as_float(s[1])); }
template <int I, int N, class Fn> __device__ __forceinline__ void static_for(Fn&& f) {
  if constexpr (I < N) { f(std::integral_constant<int, I>{}); static_for<I + 1, N>(f); }
}

struct Sel { unsigned s[4]; };
__device__ __forceinline__ Sel make_sel(int lane) {
  const int n = lane & 15;
  const unsigned src = (n & 8) ? 0x0302u : 0x0100u;
  const unsigned one = (n & 1) ? ((src << 16) | 0x0c0cu) : (0x0c0c0000u | src);
  Sel q;
#pragma unroll
  for (int d = 0; d < 4; ++d) q.s[d] = (((n & 7) >> 1) == d) ? one : 0x0c0c0c0cu;
  return q;
}
__device__ __forceinline__ v4u make_b(unsigned p2, const Sel& q) {
  return v4u{__builtin_amdgcn_perm(p2, p2, q.s[0]), __builtin_amdgcn_perm(p2, p2, q.s[1]), __builtin_amdgcn_perm(p2, p2, q.s[2]), __builtin_amdgcn_perm(p2, p2, q.s[3])};
}
__device__ __forceinline__ f32x4 mfma(v4u a, v4u b, f32x4 c) {
  return __builtin_amdgcn_mfma_f32_16x16x32_bf16(__builtin_bit_cast(bf16x8, a), __builtin_bit_cast(bf16x8, b), c, 0, 0, 0);
}

struct Tag { unsigned hi, lo; };
__device__ __forceinline__ u64 enc(float v, const Tag& t) { const unsigned p = (__float_as_uint(v) + 0x80u) >> 8; return ((u64)t.hi << 32) | (u64)(p | t.lo); }
__device__ __forceinline__ unsigned bad(u64 g, const Tag& t) { return ((unsigned)(g >> 32) ^ t.hi) | (((unsigned)g ^ t.lo) >> 24); }
__device__ __forceinline__ float dec(u64 g) { return __uint_as_float((unsigned)g << 8); }
__device__ __forceinline__ u64 gld(const unsigned char* ws, size_t off) { return __hip_atomic_load((const GAS u64*)(ws + off), __ATOMIC_RELAXED, __HIP_MEMORY_SCOPE_AGENT); }
__device__ __forceinline__ void gst(unsigned char* ws, size_t off, u64 v) { __hip_atomic_store((GAS u64*)(ws + off), v, __ATOMIC_RELAXED, __HIP_MEMORY_SCOPE_AGENT); }

__device__ __forceinline__ void lds_wait_gt(volatile LAS unsigned* p, unsigned v) {
  while ((unsigned)__builtin_amdgcn_readfirstlane(*p) <= v) {}
  asm volatile("" ::: "memory");
}
__device__ __forceinline__ unsigned lds_arrive(LAS unsigned* p, int lane) {
  asm volatile("s_waitcnt lgkmcnt(0)" ::: "memory");
  unsigned old = 0;
  if (lane == 0) old = __hip_atomic_fetch_add(p, 1u, __ATOMIC_RELAXED, __HIP_MEMORY_SCOPE_WORKGROUP);
  old = __builtin_amdgcn_readfirstlane(old);
  asm volatile("" ::: "memory");
  return old;
}

#define NTS " nt"
__device__ __forceinline__ void dma4(const void* sbase, unsigned voff, unsigned ldsaddr) {
  unsigned keep;
  asm volatile("s_mov_b32 %0, m0\n\ts_mov_b32 m0, %3\n\ts_nop 0\n\t"
               "global_load_lds_dwordx4 %1, %2" NTS "\n\t"
               "global_load_lds_dwordx4 %1, %2 offset:1024" NTS "\n\t"
               "global_load_lds_dwordx4 %1, %2 offset:2048" NTS "\n\t"
               "global_load_lds_dwordx4 %1, %2 offset:3072" NTS "\n\t"
               "s_mov_b32 m0, %0"
               : "=&s"(keep) : "v"(voff), "s"(sbase), "s"(ldsaddr) : "memory");
}
__device__ __forceinline__ void dma1(const void* sbase, unsigned voff, unsigned ldsaddr) {
  unsigned keep;
  asm volatile("s_mov_b32 %0, m0\n\ts_mov_b32 m0, %3\n\ts_nop 0\n\t"
               "global_load_lds_dwordx4 %1, %2" NTS "\n\t"
               "s_mov_b32 m0, %0"
               : "=&s"(keep) : "v"(voff), "s"(sbase), "s"(ldsaddr) : "memory");
}
template <int N> __device__ __forceinline__ void wait_vm() { asm volatile("s_waitcnt vmcnt(%0)" :: "n"(N) : "memory"); }

#define STAMP(i) do {} while (0)

__global__ void __launch_bounds__(NTHR, 2) fused_decode_kernel(Params p) {
  extern __shared__ __attribute__((aligned(16))) unsigned char smem[];
  LAS unsigned char* L = (LAS unsigned char*)smem;
  volatile LAS unsigned* flags = (volatile LAS unsigned*)(L + FLAGS);
  const int tid = threadIdx.x, lane = tid & 63, wave = __builtin_amdgcn_readfirstlane(tid >> 6);
  const int bid = blockIdx.x;
  const int g = (bid & 7) * 4 + (bid >> 6), j = (bid >> 3) & 7;
  const int bb = j >> 2, sp = j & 3;
  const int cu = g * 8 + j;
  STAMP(0);

  if (wave == 0) {
    if (lane < 32) flags[lane] = 0u;
    asm volatile("s_waitcnt lgkmcnt(0)" ::: "memory");
    __builtin_amdgcn_s_barrier();
    __builtin_amdgcn_s_setprio(2);
    const size_t kvoff = ((size_t)(bb * H + g) * SK + sp * 1024) * DH * 2;
    const unsigned char* kb = (const unsigned char*)p.kc + kvoff;
    const unsigned char* vb = (const unsigned char*)p.vc + kvoff;
    const unsigned voff = lane * 16;
    const unsigned lbase = (unsigned)(uintptr_t)L;
    int slot = 0;
#pragma unroll 1
    for (int f = 0; f < NFILL; ++f) {
      if (f >= NSLOT) {
        const unsigned need = (unsigned)(f - NSLOT + 1);
        for (;;) {
          const v4u fr = *(volatile LAS v4u*)(L + FLAGS + F_FREE * 4);
          const unsigned mn = min(min(fr.x, fr.y), min(fr.z, fr.w));
          if ((unsigned)__builtin_amdgcn_readfirstlane(mn) >= need) break;
          __builtin_amdgcn_s_sleep(1);
        }
      }
      const unsigned char* src = (f < 16 ? kb : vb - 16 * SLOT) + (size_t)f * SLOT;
      const unsigned dst = lbase + slot * SLOT;
#pragma unroll
      for (int q4 = 0; q4 < 4; ++q4) dma4(src + q4 * 4096, voff, dst + q4 * 4096);
      slot = slot + 1 == NSLOT ? 0 : slot + 1;
      if (f >= 1) { wait_vm<16>(); flags[F_FULL] = (unsigned)f; }
      if (f == 0) STAMP(1);
      if (f == 16) STAMP(2);
    }
    STAMP(3);
    for (;;) {
      const v4u fr = *(volatile LAS v4u*)(L + FLAGS + F_FREE * 4);
      const unsigned mn = min(min(fr.x, fr.y), min(fr.z, fr.w));
      if ((unsigned)__builtin_amdgcn_readfirstlane(mn) >= W_NEED_FREE) break;
      __builtin_amdgcn_s_sleep(1);
    }
    const int rr = lane / 9, cc = lane - rr * 9;
    const unsigned woff = rr * (D * 2) + cc * 16;
    const unsigned char* w2b = (const unsigned char*)p.w2 + ((size_t)g * 144 * D + j * 72) * 2;
    const unsigned char* wob = (const unsigned char*)p.wo + ((size_t)g * 128 * D + j * 72) * 2;
    if (lane < 63) {
#pragma unroll 1
      for (int i = 0; i < 20; ++i) dma1(w2b + (size_t)i * 7 * D * 2, woff, lbase + W2_OFF + i * 1008);
    }
    if (lane < 36) dma1(w2b + (size_t)20 * 7 * D * 2, woff, lbase + W2_OFF + 20 * 1008);
    wait_vm<21>(); flags[F_FULL] = (unsigned)NFILL;
    if (lane < 63) {
#pragma unroll 1
      for (int i = 0; i < 18; ++i) dma1(wob + (size_t)i * 7 * D * 2, woff, lbase + WO_OFF + i * 1008);
    }
    if (lane < 18) dma1(wob + (size_t)18 * 7 * D * 2, woff, lbase + WO_OFF + 18 * 1008);
    wait_vm<19>(); flags[F_FULL] = (unsigned)NFILL + 1;
    wait_vm<0>(); flags[F_FULL] = (unsigned)NFILL + 2;
    STAMP(4);
    return;
  }

  const int k = wave - 1;
  const int n = lane & 15, r = lane >> 4;
  const u64 E = gld(p.ws, WS_E);
  unsigned xs[9];
  {
    const GAS unsigned* xd = (const GAS unsigned*)p.x + (lane >> 5) * (D / 2) + (lane & 31);
#pragma unroll
    for (int t = 0; t < 9; ++t) xs[t] = xd[32 * t];
  }
  const int row0 = 72 * j + 18 * k;
  unsigned xv[5]; v4u aq[5];
#pragma unroll
  for (int u = 0; u < 5; ++u) {
    const int rl = min(4 * u + r, 17);
    xv[u] = ((const GAS u16*)p.x)[(n >> 3) * D + row0 + rl];
    aq[u] = __builtin_nontemporal_load((const GAS v4u*)(p.wq + (size_t)(row0 + rl) * HD + g * DH + 8 * n));
  }
  __builtin_amdgcn_sched_barrier(0);
  __builtin_amdgcn_s_barrier();
  __builtin_amdgcn_sched_barrier(0);
  v4u ah[2][5];
#pragma unroll
  for (int u = 0; u < 5; ++u) {
    const int rl = min(4 * u + r, 17);
#pragma unroll
    for (int hf = 0; hf < 2; ++hf)
      ah[hf][u] = __builtin_nontemporal_load((const GAS v4u*)(p.w1 + (size_t)(row0 + rl) * F + g * 144 + hf * 72 + 8 * min(n, 8)));
  }
  const unsigned b1v = ((const GAS u16*)p.b1)[g * 144 + 36 * k + min(lane, 35)];
  const int rho = min(g, 17), rb = rho / 9, ri = rho - rb * 9;
  const int ocol = 72 * j + 8 * ri + (lane & 7);
  const unsigned xres = ((const GAS u16*)p.x)[rb * D + ocol];
  const unsigned b2v = ((const GAS u16*)p.b2)[ocol];
  __builtin_amdgcn_sched_barrier(0);

  const Sel sel = make_sel(lane);
  float mb, den;
  {
    float s = 0.f;
#pragma unroll
    for (int t = 0; t < 9; ++t) s += bf2f(xs[t] & 0xffffu) + __uint_as_float(xs[t] & 0xffff0000u);
    s = x16_sum(sum16(s));
    const float mean = s * (1.0f / D);
    float ss = 0.f;
#pragma unroll
    for (int t = 0; t < 9; ++t) { const float a = bf2f(xs[t] & 0xffffu) - mean, b = __uint_as_float(xs[t] & 0xffff0000u) - mean; ss = fmaf(a, a, ss); ss = fmaf(b, b, ss); }
    ss = x16_sum(sum16(ss));
    const float m_ = rbf(mean), d_ = rbf(sqrtf(rbf(rbf(ss * (1.0f / D)) + 1e-5f)));
    auto sm = __builtin_amdgcn_permlane32_swap(__float_as_uint(m_), __float_as_uint(m_), false, false);
    auto sd = __builtin_amdgcn_permlane32_swap(__float_as_uint(d_), __float_as_uint(d_), false, false);
    mb = __uint_as_float((n & 8) ? sm[1] : sm[0]);
    den = __uint_as_float((n & 8) ? sd[1] : sd[0]);
  }
  v4u bx[5];
#pragma unroll
  for (int u = 0; u < 5; ++u) {
    unsigned v = f2bf(rbf(bf2f(xv[u]) - mb) / den);
    v = (4 * u + r < 18) ? v : 0u;
    bx[u] = make_b(v | (v << 16), sel);
  }
  Tag tag;
  const unsigned Elo = __builtin_amdgcn_readfirstlane((unsigned)E), Ehi = __builtin_amdgcn_readfirstlane((unsigned)(E >> 32));
  const u64 Eu = ((u64)Ehi << 32) | Elo;
  {
    u64 z = Eu + 0x9E3779B97F4A7C15ull;
    z = (z ^ (z >> 30)) * 0xBF58476D1CE4E5B9ull;
    z = (z ^ (z >> 27)) * 0x94D049BB133111EBull;
    z ^= z >> 31;
    tag.hi = (unsigned)(z >> 32); tag.lo = (unsigned)z & 0xff000000u;
  }
  STAMP(1);
  {
    f32x4 acc = {0.f, 0.f, 0.f, 0.f};
#pragma unroll
    for (int u = 0; u < 5; ++u) acc = mfma(aq[u], bx[u], acc);
    *(LAS f32x4*)(L + TILE_A + k * 1024 + lane * 16) = acc;
    if (lds_arrive((LAS unsigned*)(L + FLAGS) + F_CQ, lane) == 3) {
      f32x4 t = *(LAS f32x4*)(L + TILE_A + lane * 16);
#pragma unroll
      for (int w = 1; w < 4; ++w) t += *(LAS f32x4*)(L + TILE_A + w * 1024 + lane * 16);
#pragma unroll
      for (int i = 0; i < 4; ++i) gst(p.ws, WS_QP + ((size_t)cu * 256 + i * 64 + lane) * 8, enc(t[i], tag));
    }
  }
  STAMP(2);
  {
    f32x4 a0 = {0.f, 0.f, 0.f, 0.f}, a1 = {0.f, 0.f, 0.f, 0.f};
#pragma unroll
    for (int u = 0; u < 5; ++u) { a0 = mfma(ah[0][u], bx[u], a0); a1 = mfma(ah[1][u], bx[u], a1); }
    *(LAS f32x4*)(L + TILE_B + k * 2048 + lane * 16) = a0;
    *(LAS f32x4*)(L + TILE_B + k * 2048 + 1024 + lane * 16) = a1;
    if (lds_arrive((LAS unsigned*)(L + FLAGS) + F_CH, lane) == 3) {
#pragma unroll
      for (int hf = 0; hf < 2; ++hf) {
        f32x4 t = *(LAS f32x4*)(L + TILE_B + hf * 1024 + lane * 16);
#pragma unroll
        for (int w = 1; w < 4; ++w) t += *(LAS f32x4*)(L + TILE_B + w * 2048 + hf * 1024 + lane * 16);
#pragma unroll
        for (int i = 0; i < 4; ++i) if (4 * r + i < 9) gst(p.ws, WS_HP + ((size_t)cu * 512 + hf * 256 + i * 64 + lane) * 8, enc(t[i], tag));
      }
    }
  }
  STAMP(3);
  v4u qv;
  {
    const size_t off = WS_QP + ((size_t)(g * 8 + (lane >> 5)) * 256 + ((lane >> 3) & 3) * 64 + k * 16 + bb * 8 + (lane & 7)) * 8;
    u64 gq[4];
    for (unsigned spin = 0;; ++spin) {
      unsigned bd = 0;
#pragma unroll
      for (int u = 0; u < 4; ++u) gq[u] = gld(p.ws, off + (size_t)u * 2 * 256 * 8);
#pragma unroll
      for (int u = 0; u < 4; ++u) bd |= bad(gq[u], tag);
      if (__all(bd == 0) || spin > SPIN_CAP) break;
      __builtin_amdgcn_s_sleep(2);
    }
    const float q32 = x32_sum((dec(gq[0]) + dec(gq[1])) + (dec(gq[2]) + dec(gq[3])));
    if (lane < 32) *(LAS u16*)(L + QB + (32 * k + lane) * 2) = (u16)f2bf(q32);
    lds_arrive((LAS unsigned*)(L + FLAGS) + F_CQR, lane);
    lds_wait_gt(flags + F_CQR, 3);
    qv = *(LAS v4u*)(L + QB + n * 16);
  }
  STAMP(4);
  float sc0 = 0.f, sc1 = 0.f, sc2 = 0.f, sc3 = 0.f;
  int slot = 0;
  const LAS unsigned char* ringw = L + k * 4096 + lane * 16;
#pragma unroll 1
  for (int grp = 0; grp < 4; ++grp) {
    float xx[16];
#pragma unroll
    for (int sl = 0; sl < 4; ++sl) {
      const unsigned f = grp * 4 + sl;
      lds_wait_gt(flags + F_FULL, f);
      const LAS v4u* src = (const LAS v4u*)(ringw + slot * SLOT);
      v4u a0 = src[0], a1 = src[64], a2 = src[128], a3 = src[192];
      asm volatile("" : "+v"(a0), "+v"(a1), "+v"(a2), "+v"(a3) :: "memory");
      flags[F_FREE + k] = f + 1;
      asm volatile("" : "+v"(a0), "+v"(a1), "+v"(a2), "+v"(a3) :: "memory");
      xx[sl * 4 + 0] = dot2(a0.w, qv.w, dot2(a0.z, qv.z, dot2(a0.y, qv.y, dot2(a0.x, qv.x, 0.f))));
      xx[sl * 4 + 1] = dot2(a1.w, qv.w, dot2(a1.z, qv.z, dot2(a1.y, qv.y, dot2(a1.x, qv.x, 0.f))));
      xx[sl * 4 + 2] = dot2(a2.w, qv.w, dot2(a2.z, qv.z, dot2(a2.y, qv.y, dot2(a2.x, qv.x, 0.f))));
      xx[sl * 4 + 3] = dot2(a3.w, qv.w, dot2(a3.z, qv.z, dot2(a3.y, qv.y, dot2(a3.x, qv.x, 0.f))));
      slot = slot + 1 == NSLOT ? 0 : slot + 1;
    }
#define TR_STEP(HALF, CTRL, BIT) _Pragma("unroll") for (int hh = 0; hh < (HALF); ++hh) { \
      const float lo_ = xx[hh], hi_ = xx[hh + (HALF)]; const float t1_ = lo_ + dpp<CTRL>(lo_), t2_ = hi_ + dpp<CTRL>(hi_); \
      xx[hh] = (n & (BIT)) ? t2_ : t1_; }
    TR_STEP(8, XOR8, 8) TR_STEP(4, XOR7, 4) TR_STEP(2, XOR2, 2) TR_STEP(1, XOR1, 1)
#undef TR_STEP
    sc0 = sc1; sc1 = sc2; sc2 = sc3;
    sc3 = rbf(rbf(xx[0]) * 0.08838834764831845f);
  }
  STAMP(5);
  float M;
  {
    float m = fmaxf(fmaxf(sc0, sc1), fmaxf(sc2, sc3));
    m = x32_max(x16_max(max16(m)));
    if (lane == 0) flags[F_MW + k] = __float_as_uint(m);
    lds_arrive((LAS unsigned*)(L + FLAGS) + F_CM, lane);
    lds_wait_gt(flags + F_CM, 3);
    const v4u mw = *(volatile LAS v4u*)(L + FLAGS + F_MW * 4);
    M = fmaxf(fmaxf(__uint_as_float(mw.x), __uint_as_float(mw.y)), fmaxf(__uint_as_float(mw.z), __uint_as_float(mw.w)));
  }
  constexpr float LOG2E = 1.4426950408889634f;
  unsigned P0, P1, P2, P3; float lsum;
  {
    const float p0 = __builtin_amdgcn_exp2f((sc0 - M) * LOG2E), p1 = __builtin_amdgcn_exp2f((sc1 - M) * LOG2E);
    const float p2 = __builtin_amdgcn_exp2f((sc2 - M) * LOG2E), p3 = __builtin_amdgcn_exp2f((sc3 - M) * LOG2E);
    lsum = x32_sum(x16_sum(sum16((p0 + p1) + (p2 + p3))));
    auto pk = [](float v) { const unsigned hi = f2bf(v); const unsigned lo = f2bf(v - bf2f(hi)); return hi | (lo << 16); };
    P0 = pk(p0); P1 = pk(p1); P2 = pk(p2); P3 = pk(p3);
  }
  {
    const int U = 36 * k + min(lane, 35);
    const int hf = U >= 72 ? 1 : 0, ul = U - 72 * hf, ci = ul >> 3;
    const size_t off = WS_HP + ((size_t)g * 8 * 512 + hf * 256 + (ci & 3) * 64 + (ci >> 2) * 16 + (ul & 7)) * 8;
    u64 gh[16];
    for (unsigned spin = 0;; ++spin) {
      unsigned bd = 0;
#pragma unroll
      for (int u = 0; u < 16; ++u) gh[u] = gld(p.ws, off + ((size_t)(u >> 1) * 512 + (u & 1) * 8) * 8);
#pragma unroll
      for (int u = 0; u < 16; ++u) bd |= bad(gh[u], tag);
      if (__all(bd == 0) || spin > SPIN_CAP) break;
      __builtin_amdgcn_s_sleep(2);
    }
    float h0 = 0.f, h1 = 0.f;
#pragma unroll
    for (int u = 0; u < 8; ++u) { h0 += dec(gh[2 * u]); h1 += dec(gh[2 * u + 1]); }
    const float bias = bf2f(b1v);
    h0 = fmaxf(rbf(rbf(h0) + bias), 0.f); h1 = fmaxf(rbf(rbf(h1) + bias), 0.f);
    const unsigned x2 = f2bf(h0 * h0) | (f2bf(h1 * h1) << 16);
    if (lane < 36) *(LAS unsigned*)(L + X2H + (36 * k + lane) * 4) = x2;
  }
  STAMP(6);
  f32x4 oacc = {0.f, 0.f, 0.f, 0.f};
#pragma unroll 1
  for (int grp = 0; grp < 4; ++grp) {
    const unsigned P = P0; P0 = P1; P1 = P2; P2 = P3;
    static_for<0, 4>([&](auto slc) {
      constexpr int sl = decltype(slc)::value;
      const unsigned f = 16 + grp * 4 + sl;
      lds_wait_gt(flags + F_FULL, f);
      const LAS v4u* src = (const LAS v4u*)(ringw + slot * SLOT);
      v4u a0 = src[0], a1 = src[64], a2 = src[128], a3 = src[192];
      asm volatile("" : "+v"(a0), "+v"(a1), "+v"(a2), "+v"(a3) :: "memory");
      flags[F_FREE + k] = f + 1;
      asm volatile("" : "+v"(a0), "+v"(a1), "+v"(a2), "+v"(a3) :: "memory");
      oacc = mfma(a0, make_b(rowbcast<sl * 4 + 0>(P), sel), oacc);
      oacc = mfma(a1, make_b(rowbcast<sl * 4 + 1>(P), sel), oacc);
      oacc = mfma(a2, make_b(rowbcast<sl * 4 + 2>(P), sel), oacc);
      oacc = mfma(a3, make_b(rowbcast<sl * 4 + 3>(P), sel), oacc);
      slot = slot + 1 == NSLOT ? 0 : slot + 1;
    });
  }
  STAMP(7);
  {
#pragma unroll
    for (int i = 0; i < 4; ++i) oacc[i] += dpp<XOR8>(oacc[i]);
    *(LAS f32x4*)(L + TILE_A + k * 1024 + lane * 16) = oacc;
    if (lane == 0) flags[F_LW + k] = __float_as_uint(lsum);
    if (lds_arrive((LAS unsigned*)(L + FLAGS) + F_CO, lane) == 3) {
      f32x4 t = *(LAS f32x4*)(L + TILE_A + lane * 16);
#pragma unroll
      for (int w = 1; w < 4; ++w) t += *(LAS f32x4*)(L + TILE_A + w * 1024 + lane * 16);
      const v4u lw = *(volatile LAS v4u*)(L + FLAGS + F_LW * 4);
      const float lt = (__uint_as_float(lw.x) + __uint_as_float(lw.y)) + (__uint_as_float(lw.z) + __uint_as_float(lw.w));
      const int hi = n >> 3;
      const float v0 = hi ? t[2] : t[0], v1 = hi ? t[3] : t[1];
      const size_t ob = WS_OP + (size_t)cu * 136 * 8;
      gst(p.ws, ob + (32 * r + 16 * hi + (n & 7)) * 8, enc(v0, tag));
      gst(p.ws, ob + (32 * r + 16 * hi + 8 + (n & 7)) * 8, enc(v1, tag));
      if (lane < 2) gst(p.ws, ob + (128 + lane) * 8, enc(lane ? lt : M, tag));
    }
  }
  STAMP(8);
  f32x4 facc = {0.f, 0.f, 0.f, 0.f};
  {
    lds_wait_gt(flags + F_FULL, NFILL);
    const LAS unsigned char* wb = L + W2_OFF + (36 * k + r) * 144 + min(n, 8) * 16;
    const LAS unsigned char* xb = L + X2H + (36 * k + r) * 4;
#pragma unroll
    for (int u = 0; u < 9; ++u) facc = mfma(*(const LAS v4u*)(wb + u * 4 * 144), make_b(*(const LAS unsigned*)(xb + u * 16), sel), facc);
  }
  STAMP(9);
  f32x4 sacc = {0.f, 0.f, 0.f, 0.f};
  {
    const int ob_ = lane >> 5;
    const size_t off = WS_OP + ((size_t)(g * 8 + ob_ * 4) * 136 + 32 * k + (lane & 31)) * 8;
    const size_t offm = WS_OP + ((size_t)(g * 8 + ob_ * 4) * 136 + 128) * 8;
    u64 go[4], gm[4], gl[4];
    for (unsigned spin = 0;; ++spin) {
      unsigned bd = 0;
#pragma unroll
      for (int u = 0; u < 4; ++u) { go[u] = gld(p.ws, off + (size_t)u * 136 * 8); gm[u] = gld(p.ws, offm + (size_t)u * 136 * 8); gl[u] = gld(p.ws, offm + (size_t)u * 136 * 8 + 8); }
#pragma unroll
      for (int u = 0; u < 4; ++u) bd |= bad(go[u], tag) | bad(gm[u], tag) | bad(gl[u], tag);
      if (__all(bd == 0) || spin > SPIN_CAP) break;
      __builtin_amdgcn_s_sleep(1);
    }
    const float Mx = fmaxf(fmaxf(dec(gm[0]), dec(gm[1])), fmaxf(dec(gm[2]), dec(gm[3])));
    float Ls = 0.f, os = 0.f;
#pragma unroll
    for (int u = 0; u < 4; ++u) { const float w = __builtin_amdgcn_exp2f((dec(gm[u]) - Mx) * LOG2E); Ls = fmaf(w, dec(gl[u]), Ls); os = fmaf(w, dec(go[u]), os); }
    const unsigned ob16 = f2bf(os / Ls);
    auto sw = __builtin_amdgcn_permlane32_swap(ob16, ob16, false, false);
    if (lane < 32) *(LAS unsigned*)(L + X2O + (32 * k + lane) * 4) = sw[0] | (sw[1] << 16);
    STAMP(10);
    lds_wait_gt(flags + F_FULL, NFILL + 1);
    const LAS unsigned char* wb = L + WO_OFF + (32 * k + r) * 144 + min(n, 8) * 16;
    const LAS unsigned char* xb = L + X2O + (32 * k + r) * 4;
#pragma unroll
    for (int u = 0; u < 8; ++u) sacc = mfma(*(const LAS v4u*)(wb + u * 4 * 144), make_b(*(const volatile LAS unsigned*)(xb + u * 16), sel), sacc);
  }
  STAMP(11);
  *(LAS f32x4*)(L + TILE_B + k * 2048 + lane * 16) = sacc;
  *(LAS f32x4*)(L + TILE_B + k * 2048 + 1024 + lane * 16) = facc;
  if (lds_arrive((LAS unsigned*)(L + FLAGS) + F_CF, lane) != 3) return;
  {
    f32x4 ts = *(LAS f32x4*)(L + TILE_B + lane * 16), tf = *(LAS f32x4*)(L + TILE_B + 1024 + lane * 16);
#pragma unroll
    for (int w = 1; w < 4; ++w) { ts += *(LAS f32x4*)(L + TILE_B + w * 2048 + lane * 16); tf += *(LAS f32x4*)(L + TILE_B + w * 2048 + 1024 + lane * 16); }
#pragma unroll
    for (int i = 0; i < 4; ++i) if (4 * r + i < 9) {
      const size_t o = WS_FIN + ((((size_t)j * 18 + (n >> 3) * 9 + 4 * r + i) * 32 + g) * 16 + (n & 7)) * 8;
      gst(p.ws, o, enc(ts[i], tag)); gst(p.ws, o + 64, enc(tf[i], tag));
    }
  }
  STAMP(12);
  if (g >= 18) return;
  {
    const size_t off = WS_FIN + ((((size_t)j * 18 + g) * 32 + r) * 16 + n) * 8;
    u64 gf[8];
    for (unsigned spin = 0;; ++spin) {
      unsigned bd = 0;
#pragma unroll
      for (int u = 0; u < 8; ++u) gf[u] = gld(p.ws, off + (size_t)u * 4 * 16 * 8);
#pragma unroll
      for (int u = 0; u < 8; ++u) bd |= bad(gf[u], tag);
      if (__all(bd == 0) || spin > SPIN_CAP) break;
      __builtin_amdgcn_s_sleep(1);
    }
    float t = 0.f;
#pragma unroll
    for (int u = 0; u < 8; ++u) t += dec(gf[u]);
    t = x32_sum(x16_sum(t));
    const float other = dpp<XOR8>(t);
    const float s = rbf(t);
    const float ff = rbf(rbf(other) + bf2f(b2v));
    const float gate = rbf(ff / (1.0f + __expf(-ff)));
    const unsigned o16 = f2bf(bf2f(xres) + rbf(s * gate));
    if (lane < 8) ((GAS u16*)p.out)[rb * D + ocol] = (u16)o16;
    if (bid == 0 && lane == 0) gst(p.ws, WS_E, Eu + 1);
  }
  STAMP(13);
}

}

extern "C" void kernel_launch(void* const* d_in, const int* in_sizes, int n_in, void* d_out, int out_size, void* d_ws, size_t ws_size, hipStream_t stream) {
  Params p;
  p.x = (const u16*)d_in[0]; p.kc = (const u16*)d_in[1]; p.vc = (const u16*)d_in[2]; p.wq = (const u16*)d_in[3]; p.wo = (const u16*)d_in[4];
  p.w1 = (const u16*)d_in[6]; p.b1 = (const u16*)d_in[7]; p.w2 = (const u16*)d_in[8]; p.b2 = (const u16*)d_in[9];
  p.out = (u16*)d_out; p.ws = (unsigned char*)d_ws;
  hipFuncSetAttribute((const void*)fused_decode_kernel, hipFuncAttributeMaxDynamicSharedMemorySize, LDS_BYTES);
  hipLaunchKernelGGL(fused_decode_kernel, dim3(NWG), dim3(NTHR), LDS_BYTES, stream, p);
}
```

```cpp
#include <hip/hip_runtime.h>
#include <hip/hip_bf16.h>
#include <stdint.h>
#include <type_traits>

namespace {

#define GAS __attribute__((address_space(1)))
#define LAS __attribute__((address_space(3)))
typedef unsigned v4u __attribute__((ext_vector_type(4)));
typedef float f32x4 __attribute__((ext_vector_type(4)));
typedef __bf16 bf16x2 __attribute__((ext_vector_type(2)));
typedef __bf16 bf16x8 __attribute__((ext_vector_type(8)));
typedef unsigned long long u64;
typedef unsigned short u16;

constexpr int D = 576, H = 32, SK = 4096, DH = 128, F = 4608, HD = H * DH;
constexpr int NWG = 256, NTHR = 320;

#define NSLOT 4
#define KV_NT 1
constexpr int SLOT = 16384;
constexpr int NFILL = 32;
constexpr int MISC = NSLOT * SLOT;
constexpr int TILE_A = MISC;
constexpr int TILE_B = MISC + 4096;
constexpr int QB = MISC + 12288;
constexpr int X2H = QB + 256;
constexpr int X2O = X2H + 576;
constexpr int FLAGS = X2O + 512;
constexpr int LDS_BYTES = FLAGS + 128;
static_assert(LDS_BYTES <= 163840, "LDS");
static_assert((FLAGS & 15) == 0, "align");
constexpr int WSLOT = NFILL % NSLOT;
static_assert(WSLOT + 2 < NSLOT, "w region must not wrap");
constexpr int W2_OFF = WSLOT * SLOT, WO_OFF = W2_OFF + 144 * 144;
constexpr unsigned W_NEED_FREE = NFILL + 3 - NSLOT;
enum { F_FULL = 0, F_CQ = 1, F_CH = 2, F_CQR = 3, F_FREE = 4  , F_MW = 8  , F_LW = 12  , F_CM = 16, F_CO = 17, F_CF = 18 };

constexpr size_t WS_E = 0;
constexpr size_t WS_QP = 4096;
constexpr size_t WS_HP = WS_QP + (size_t)NWG * 256 * 8;
constexpr size_t WS_OP = WS_HP + (size_t)NWG * 512 * 8;
constexpr size_t WS_FIN = WS_OP + (size_t)NWG * 136 * 8;
constexpr size_t WS_END = WS_FIN + (size_t)8 * 18 * 32 * 16 * 8;
constexpr size_t WS_STAMP = 8u << 20;
constexpr unsigned SPIN_CAP = 1u << 15;

struct Params {
  const u16* x; const u16* kc; const u16* vc; const u16* wq; const u16* wo;
  const u16* w1; const u16* b1; const u16* w2; const u16* b2; u16* out; unsigned char* ws;
};
static_assert(sizeof(Params) == 88, "no padding");

__device__ __forceinline__ float bf2f(unsigned b) { return __builtin_bit_cast(float, b << 16); }
__device__ __forceinline__ unsigned f2bf(float f) { return (unsigned)__builtin_bit_cast(u16, __float2bfloat16(f)); }
__device__ __forceinline__ float rbf(float f) { return bf2f(f2bf(f)); }
__device__ __forceinline__ float dot2(unsigned k, unsigned q, float acc) {
  return __builtin_amdgcn_fdot2_f32_bf16(__builtin_bit_cast(bf16x2, k), __builtin_bit_cast(bf16x2, q), acc, false);
}
template <int CTRL> __device__ __forceinline__ float dpp(float x) {
  return __builtin_bit_cast(float, __builtin_amdgcn_mov_dpp(__builtin_bit_cast(int, x), CTRL, 0xf, 0xf, true));
}
template <int I> __device__ __forceinline__ unsigned rowbcast(unsigned x) {
  return (unsigned)__builtin_amdgcn_update_dpp(0, (int)x, 0x150 + I, 0xf, 0xf, true);
}
constexpr int XOR1 = 0xB1, XOR2 = 0x4E, XOR7 = 0x141, XOR8 = 0x128;
__device__ __forceinline__ float sum16(float x) { x += dpp<XOR1>(x); x += dpp<XOR2>(x); x += dpp<XOR7>(x); x += dpp<XOR8>(x); return x; }
__device__ __forceinline__ float max16(float x) { x = fmaxf(x, dpp<XOR1>(x)); x = fmaxf(x, dpp<XOR2>(x)); x = fmaxf(x, dpp<XOR7>(x)); x = fmaxf(x, dpp<XOR8>(x)); return x; }
__device__ __forceinline__ float x16_sum(float x) { auto s = __builtin_amdgcn_permlane16_swap(__float_as_uint(x), __float_as_uint(x), false, false); return __uint_as_float(s[0]) + __uint_as_float(s[1]); }
__device__ __forceinline__ float x32_sum(float x) { auto s = __builtin_amdgcn_permlane32_swap(__float_as_uint(x), __float_as_uint(x), false, false); return __uint_as_float(s[0]) + __uint_as_float(s[1]); }
__device__ __forceinline__ float x16_max(float x) { auto s = __builtin_amdgcn_permlane16_swap(__float_as_uint(x), __float_as_uint(x), false, false); return fmaxf(__uint_as_float(s[0]), __uint_as_float(s[1])); }
__device__ __forceinline__ float x32_max(float x) { auto s = __builtin_amdgcn_permlane32_swap(__float_as_uint(x), __float_as_uint(x), false, false); return fmaxf(__uint_as_float(s[0]), __uint_as_float(s[1])); }
template <int I, int N, class Fn> __device__ __forceinline__ void static_for(Fn&& f) {
  if constexpr (I < N) { f(std::integral_constant<int, I>{}); static_for<I + 1, N>(f); }
}

struct Sel { unsigned s[4]; };
__device__ __forceinline__ Sel make_sel(int lane) {
  const int n = lane & 15;
  const unsigned src = (n & 8) ? 0x0302u : 0x0100u;
  const unsigned one = (n & 1) ? ((src << 16) | 0x0c0cu) : (0x0c0c0000u | src);
  Sel q;
#pragma unroll
  for (int d = 0; d < 4; ++d) q.s[d] = (((n & 7) >> 1) == d) ? one : 0x0c0c0c0cu;
  return q;
}
__device__ __forceinline__ v4u make_b(unsigned p2, const Sel& q) {
  return v4u{__builtin_amdgcn_perm(p2, p2, q.s[0]), __builtin_amdgcn_perm(p2, p2, q.s[1]), __builtin_amdgcn_perm(p2, p2, q.s[2]), __builtin_amdgcn_perm(p2, p2, q.s[3])};
}
__device__ __forceinline__ f32x4 mfma(v4u a, v4u b, f32x4 c) {
  return __builtin_amdgcn_mfma_f32_16x16x32_bf16(__builtin_bit_cast(bf16x8, a), __builtin_bit_cast(bf16x8, b), c, 0, 0, 0);
}

struct Tag { unsigned hi, lo; };
__device__ __forceinline__ u64 enc(float v, const Tag& t) { const unsigned p = (__float_as_uint(v) + 0x80u) >> 8; return ((u64)t.hi << 32) | (u64)(p | t.lo); }
__device__ __forceinline__ unsigned bad(u64 g, const Tag& t) { return ((unsigned)(g >> 32) ^ t.hi) | (((unsigned)g ^ t.lo) >> 24); }
__device__ __forceinline__ float dec(u64 g) { return __uint_as_float((unsigned)g << 8); }
__device__ __forceinline__ u64 gld(const unsigned char* ws, size_t off) { return __hip_atomic_load((const GAS u64*)(ws + off), __ATOMIC_RELAXED, __HIP_MEMORY_SCOPE_AGENT); }
__device__ __forceinline__ void gst(unsigned char* ws, size_t off, u64 v) { __hip_atomic_store((GAS u64*)(ws + off), v, __ATOMIC_RELAXED, __HIP_MEMORY_SCOPE_AGENT); }

__device__ __forceinline__ void lds_wait_gt(volatile LAS unsigned* p, unsigned v) {
  while ((unsigned)__builtin_amdgcn_readfirstlane(*p) <= v) {}
  asm volatile("" ::: "memory");
}
__device__ __forceinline__ unsigned lds_arrive(LAS unsigned* p, int lane) {
  asm volatile("s_waitcnt lgkmcnt(0)" ::: "memory");
  unsigned old = 0;
  if (lane == 0) old = __hip_atomic_fetch_add(p, 1u, __ATOMIC_RELAXED, __HIP_MEMORY_SCOPE_WORKGROUP);
  old = __builtin_amdgcn_readfirstlane(old);
  asm volatile("" ::: "memory");
  return old;
}

#define NTS " nt"
__device__ __forceinline__ void dma4(const void* sbase, unsigned voff, unsigned ldsaddr) {
  unsigned keep;
  asm volatile("s_mov_b32 %0, m0\n\ts_mov_b32 m0, %3\n\ts_nop 0\n\t"
               "global_load_lds_dwordx4 %1, %2" NTS "\n\t"
               "global_load_lds_dwordx4 %1, %2 offset:1024" NTS "\n\t"
               "global_load_lds_dwordx4 %1, %2 offset:2048" NTS "\n\t"
               "global_load_lds_dwordx4 %1, %2 offset:3072" NTS "\n\t"
               "s_mov_b32 m0, %0"
               : "=&s"(keep) : "v"(voff), "s"(sbase), "s"(ldsaddr) : "memory");
}
__device__ __forceinline__ void dma1(const void* sbase, unsigned voff, unsigned ldsaddr) {
  unsigned keep;
  asm volatile("s_mov_b32 %0, m0\n\ts_mov_b32 m0, %3\n\ts_nop 0\n\t"
               "global_load_lds_dwordx4 %1, %2" NTS "\n\t"
               "s_mov_b32 m0, %0"
               : "=&s"(keep) : "v"(voff), "s"(sbase), "s"(ldsaddr) : "memory");
}
template <int N> __device__ __forceinline__ void wait_vm() { asm volatile("s_waitcnt vmcnt(%0)" :: "n"(N) : "memory"); }

#define STAMP(i) do {} while (0)

__global__ void __launch_bounds__(NTHR, 2) fused_decode_kernel(Params p) {
  extern __shared__ __attribute__((aligned(16))) unsigned char smem[];
  LAS unsigned char* L = (LAS unsigned char*)smem;
  volatile LAS unsigned* flags = (volatile LAS unsigned*)(L + FLAGS);
  const int tid = threadIdx.x, lane = tid & 63, wave = __builtin_amdgcn_readfirstlane(tid >> 6);
  const int bid = blockIdx.x;
  const int g = (bid & 7) * 4 + (bid >> 6), j = (bid >> 3) & 7;
  const int bb = j >> 2, sp = j & 3;
  const int cu = g * 8 + j;
  const u64 t_start = __builtin_amdgcn_s_memrealtime();
  STAMP(0);

  if (wave == 0) {
    if (lane < 32) flags[lane] = 0u;
    asm volatile("s_waitcnt lgkmcnt(0)" ::: "memory");
    __builtin_amdgcn_s_barrier();
    __builtin_amdgcn_s_setprio(2);
    const size_t kvoff = ((size_t)(bb * H + g) * SK + sp * 1024) * DH * 2;
    const unsigned char* kb = (const unsigned char*)p.kc + kvoff;
    const unsigned char* vb = (const unsigned char*)p.vc + kvoff;
    const unsigned voff = lane * 16;
    const unsigned lbase = (unsigned)(uintptr_t)L;
    int slot = 0;
#pragma unroll 1
    for (int f = 0; f < NFILL; ++f) {
      if (f >= NSLOT) {
        const unsigned need = (unsigned)(f - NSLOT + 1);
        for (;;) {
          const v4u fr = *(volatile LAS v4u*)(L + FLAGS + F_FREE * 4);
          const unsigned mn = min(min(fr.x, fr.y), min(fr.z, fr.w));
          if ((unsigned)__builtin_amdgcn_readfirstlane(mn) >= need) break;
          __builtin_amdgcn_s_sleep(1);
        }
      }
      const unsigned char* src = (f < 16 ? kb : vb - 16 * SLOT) + (size_t)f * SLOT;
      const unsigned dst = lbase + slot * SLOT;
#pragma unroll
      for (int q4 = 0; q4 < 4; ++q4) dma4(src + q4 * 4096, voff, dst + q4 * 4096);
      slot = slot + 1 == NSLOT ? 0 : slot + 1;
      if (f >= 1) { wait_vm<16>(); flags[F_FULL] = (unsigned)f; }
      if (f == 0) STAMP(1);
      if (f == 16) STAMP(2);
    }
    STAMP(3);
    for (;;) {
      const v4u fr = *(volatile LAS v4u*)(L + FLAGS + F_FREE * 4);
      const unsigned mn = min(min(fr.x, fr.y), min(fr.z, fr.w));
      if ((unsigned)__builtin_amdgcn_readfirstlane(mn) >= W_NEED_FREE) break;
      __builtin_amdgcn_s_sleep(1);
    }
    const int rr = lane / 9, cc = lane - rr * 9;
    const unsigned woff = rr * (D * 2) + cc * 16;
    const unsigned char* w2b = (const unsigned char*)p.w2 + ((size_t)g * 144 * D + j * 72) * 2;
    const unsigned char* wob = (const unsigned char*)p.wo + ((size_t)g * 128 * D + j * 72) * 2;
    if (lane < 63) {
#pragma unroll 1
      for (int i = 0; i < 20; ++i) dma1(w2b + (size_t)i * 7 * D * 2, woff, lbase + W2_OFF + i * 1008);
    }
    if (lane < 36) dma1(w2b + (size_t)20 * 7 * D * 2, woff, lbase + W2_OFF + 20 * 1008);
    wait_vm<21>(); flags[F_FULL] = (unsigned)NFILL;
    if (lane < 63) {
#pragma unroll 1
      for (int i = 0; i < 18; ++i) dma1(wob + (size_t)i * 7 * D * 2, woff, lbase + WO_OFF + i * 1008);
    }
    if (lane < 18) dma1(wob + (size_t)18 * 7 * D * 2, woff, lbase + WO_OFF + 18 * 1008);
    wait_vm<19>(); flags[F_FULL] = (unsigned)NFILL + 1;
    wait_vm<0>(); flags[F_FULL] = (unsigned)NFILL + 2;
    STAMP(4);
    return;
  }

  const int k = wave - 1;
  const int n = lane & 15, r = lane >> 4;
  const u64 E = gld(p.ws, WS_E);
  unsigned xs[9];
  {
    const GAS unsigned* xd = (const GAS unsigned*)p.x + (lane >> 5) * (D / 2) + (lane & 31);
#pragma unroll
    for (int t = 0; t < 9; ++t) xs[t] = xd[32 * t];
  }
  const int row0 = 72 * j + 18 * k;
  unsigned xv[5]; v4u aq[5];
#pragma unroll
  for (int u = 0; u < 5; ++u) {
    const int rl = min(4 * u + r, 17);
    xv[u] = ((const GAS u16*)p.x)[(n >> 3) * D + row0 + rl];
    aq[u] = __builtin_nontemporal_load((const GAS v4u*)(p.wq + (size_t)(row0 + rl) * HD + g * DH + 8 * n));
  }
  __builtin_amdgcn_sched_barrier(0);
  __builtin_amdgcn_s_barrier();
  __builtin_amdgcn_sched_barrier(0);
  v4u ah[2][5];
#pragma unroll
  for (int u = 0; u < 5; ++u) {
    const int rl = min(4 * u + r, 17);
#pragma unroll
    for (int hf = 0; hf < 2; ++hf)
      ah[hf][u] = __builtin_nontemporal_load((const GAS v4u*)(p.w1 + (size_t)(row0 + rl) * F + g * 144 + hf * 72 + 8 * min(n, 8)));
  }
  const unsigned b1v = ((const GAS u16*)p.b1)[g * 144 + 36 * k + min(lane, 35)];
  const int rho = min(g, 17), rb = rho / 9, ri = rho - rb * 9;
  const int ocol = 72 * j + 8 * ri + (lane & 7);
  const unsigned xres = ((const GAS u16*)p.x)[rb * D + ocol];
  const unsigned b2v = ((const GAS u16*)p.b2)[ocol];
  __builtin_amdgcn_sched_barrier(0);

  const Sel sel = make_sel(lane);
  float mb, den;
  {
    float s = 0.f;
#pragma unroll
    for (int t = 0; t < 9; ++t) s += bf2f(xs[t] & 0xffffu) + __uint_as_float(xs[t] & 0xffff0000u);
    s = x16_sum(sum16(s));
    const float mean = s * (1.0f / D);
    float ss = 0.f;
#pragma unroll
    for (int t = 0; t < 9; ++t) { const float a = bf2f(xs[t] & 0xffffu) - mean, b = __uint_as_float(xs[t] & 0xffff0000u) - mean; ss = fmaf(a, a, ss); ss = fmaf(b, b, ss); }
    ss = x16_sum(sum16(ss));
    const float m_ = rbf(mean), d_ = rbf(sqrtf(rbf(rbf(ss * (1.0f / D)) + 1e-5f)));
    auto sm = __builtin_amdgcn_permlane32_swap(__float_as_uint(m_), __float_as_uint(m_), false, false);
    auto sd = __builtin_amdgcn_permlane32_swap(__float_as_uint(d_), __float_as_uint(d_), false, false);
    mb = __uint_as_float((n & 8) ? sm[1] : sm[0]);
    den = __uint_as_float((n & 8) ? sd[1] : sd[0]);
  }
  v4u bx[5];
#pragma unroll
  for (int u = 0; u < 5; ++u) {
    unsigned v = f2bf(rbf(bf2f(xv[u]) - mb) / den);
    v = (4 * u + r < 18) ? v : 0u;
    bx[u] = make_b(v | (v << 16), sel);
  }
  Tag tag;
  const unsigned Elo = __builtin_amdgcn_readfirstlane((unsigned)E), Ehi = __builtin_amdgcn_readfirstlane((unsigned)(E >> 32));
  const u64 Eu = ((u64)Ehi << 32) | Elo;
  {
    u64 z = Eu + 0x9E3779B97F4A7C15ull;
    z = (z ^ (z >> 30)) * 0xBF58476D1CE4E5B9ull;
    z = (z ^ (z >> 27)) * 0x94D049BB133111EBull;
    z ^= z >> 31;
    tag.hi = (unsigned)(z >> 32); tag.lo = (unsigned)z & 0xff000000u;
  }
  STAMP(1);
  {
    f32x4 acc = {0.f, 0.f, 0.f, 0.f};
#pragma unroll
    for (int u = 0; u < 5; ++u) acc = mfma(aq[u], bx[u], acc);
    *(LAS f32x4*)(L + TILE_A + k * 1024 + lane * 16) = acc;
    if (lds_arrive((LAS unsigned*)(L + FLAGS) + F_CQ, lane) == 3) {
      f32x4 t = *(LAS f32x4*)(L + TILE_A + lane * 16);
#pragma unroll
      for (int w = 1; w < 4; ++w) t += *(LAS f32x4*)(L + TILE_A + w * 1024 + lane * 16);
#pragma unroll
      for (int i = 0; i < 4; ++i) gst(p.ws, WS_QP + ((size_t)cu * 256 + i * 64 + lane) * 8, enc(t[i], tag));
    }
  }
  STAMP(2);
  {
    f32x4 a0 = {0.f, 0.f, 0.f, 0.f}, a1 = {0.f, 0.f, 0.f, 0.f};
#pragma unroll
    for (int u = 0; u < 5; ++u) { a0 = mfma(ah[0][u], bx[u], a0); a1 = mfma(ah[1][u], bx[u], a1); }
    *(LAS f32x4*)(L + TILE_B + k * 2048 + lane * 16) = a0;
    *(LAS f32x4*)(L + TILE_B + k * 2048 + 1024 + lane * 16) = a1;
    if (lds_arrive((LAS unsigned*)(L + FLAGS) + F_CH, lane) == 3) {
#pragma unroll
      for (int hf = 0; hf < 2; ++hf) {
        f32x4 t = *(LAS f32x4*)(L + TILE_B + hf * 1024 + lane * 16);
#pragma unroll
        for (int w = 1; w < 4; ++w) t += *(LAS f32x4*)(L + TILE_B + w * 2048 + hf * 1024 + lane * 16);
#pragma unroll
        for (int i = 0; i < 4; ++i) if (4 * r + i < 9) gst(p.ws, WS_HP + ((size_t)cu * 512 + hf * 256 + i * 64 + lane) * 8, enc(t[i], tag));
      }
    }
  }
  STAMP(3);
  v4u qv;
  {
    const size_t off = WS_QP + ((size_t)(g * 8 + (lane >> 5)) * 256 + ((lane >> 3) & 3) * 64 + k * 16 + bb * 8 + (lane & 7)) * 8;
    u64 gq[4];
    for (unsigned spin = 0;; ++spin) {
      unsigned bd = 0;
#pragma unroll
      for (int u = 0; u < 4; ++u) gq[u] = gld(p.ws, off + (size_t)u * 2 * 256 * 8);
#pragma unroll
      for (int u = 0; u < 4; ++u) bd |= bad(gq[u], tag);
      if (__all(bd == 0) || spin > SPIN_CAP) break;
      __builtin_amdgcn_s_sleep(2);
    }
    const float q32 = x32_sum((dec(gq[0]) + dec(gq[1])) + (dec(gq[2]) + dec(gq[3])));
    if (lane < 32) *(LAS u16*)(L + QB + (32 * k + lane) * 2) = (u16)f2bf(q32);
    lds_arrive((LAS unsigned*)(L + FLAGS) + F_CQR, lane);
    lds_wait_gt(flags + F_CQR, 3);
    qv = *(LAS v4u*)(L + QB + n * 16);
  }
  STAMP(4);
  float sc0 = 0.f, sc1 = 0.f, sc2 = 0.f, sc3 = 0.f;
  int slot = 0;
  const LAS unsigned char* ringw = L + k * 4096 + lane * 16;
#pragma unroll 1
  for (int grp = 0; grp < 4; ++grp) {
    float xx[16];
#pragma unroll
    for (int sl = 0; sl < 4; ++sl) {
      const unsigned f = grp * 4 + sl;
      lds_wait_gt(flags + F_FULL, f);
      const LAS v4u* src = (const LAS v4u*)(ringw + slot * SLOT);
      v4u a0 = src[0], a1 = src[64], a2 = src[128], a3 = src[192];
      asm volatile("" : "+v"(a0), "+v"(a1), "+v"(a2), "+v"(a3) :: "memory");
      flags[F_FREE + k] = f + 1;
      asm volatile("" : "+v"(a0), "+v"(a1), "+v"(a2), "+v"(a3) :: "memory");
      xx[sl * 4 + 0] = dot2(a0.w, qv.w, dot2(a0.z, qv.z, dot2(a0.y, qv.y, dot2(a0.x, qv.x, 0.f))));
      xx[sl * 4 + 1] = dot2(a1.w, qv.w, dot2(a1.z, qv.z, dot2(a1.y, qv.y, dot2(a1.x, qv.x, 0.f))));
      xx[sl * 4 + 2] = dot2(a2.w, qv.w, dot2(a2.z, qv.z, dot2(a2.y, qv.y, dot2(a2.x, qv.x, 0.f))));
      xx[sl * 4 + 3] = dot2(a3.w, qv.w, dot2(a3.z, qv.z, dot2(a3.y, qv.y, dot2(a3.x, qv.x, 0.f))));
      slot = slot + 1 == NSLOT ? 0 : slot + 1;
    }
#define TR_STEP(HALF, CTRL, BIT) _Pragma("unroll") for (int hh = 0; hh < (HALF); ++hh) { \
      const float lo_ = xx[hh], hi_ = xx[hh + (HALF)]; const float t1_ = lo_ + dpp<CTRL>(lo_), t2_ = hi_ + dpp<CTRL>(hi_); \
      xx[hh] = (n & (BIT)) ? t2_ : t1_; }
    TR_STEP(8, XOR8, 8) TR_STEP(4, XOR7, 4) TR_STEP(2, XOR2, 2) TR_STEP(1, XOR1, 1)
#undef TR_STEP
    sc0 = sc1; sc1 = sc2; sc2 = sc3;
    sc3 = rbf(rbf(xx[0]) * 0.08838834764831845f);
  }
  STAMP(5);
  float M;
  {
    float m = fmaxf(fmaxf(sc0, sc1), fmaxf(sc2, sc3));
    m = x32_max(x16_max(max16(m)));
    if (lane == 0) flags[F_MW + k] = __float_as_uint(m);
    lds_arrive((LAS unsigned*)(L + FLAGS) + F_CM, lane);
    lds_wait_gt(flags + F_CM, 3);
    const v4u mw = *(volatile LAS v4u*)(L + FLAGS + F_MW * 4);
    M = fmaxf(fmaxf(__uint_as_float(mw.x), __uint_as_float(mw.y)), fmaxf(__uint_as_float(mw.z), __uint_as_float(mw.w)));
  }
  constexpr float LOG2E = 1.4426950408889634f;
  unsigned P0, P1, P2, P3; float lsum;
  {
    const float p0 = __builtin_amdgcn_exp2f((sc0 - M) * LOG2E), p1 = __builtin_amdgcn_exp2f((sc1 - M) * LOG2E);
    const float p2 = __builtin_amdgcn_exp2f((sc2 - M) * LOG2E), p3 = __builtin_amdgcn_exp2f((sc3 - M) * LOG2E);
    lsum = x32_sum(x16_sum(sum16((p0 + p1) + (p2 + p3))));
    auto pk = [](float v) { const unsigned hi = f2bf(v); const unsigned lo = f2bf(v - bf2f(hi)); return hi | (lo << 16); };
    P0 = pk(p0); P1 = pk(p1); P2 = pk(p2); P3 = pk(p3);
  }
  {
    const int U = 36 * k + min(lane, 35);
    const int hf = U >= 72 ? 1 : 0, ul = U - 72 * hf, ci = ul >> 3;
    const size_t off = WS_HP + ((size_t)g * 8 * 512 + hf * 256 + (ci & 3) * 64 + (ci >> 2) * 16 + (ul & 7)) * 8;
    u64 gh[16];
    for (unsigned spin = 0;; ++spin) {
      unsigned bd = 0;
#pragma unroll
      for (int u = 0; u < 16; ++u) gh[u] = gld(p.ws, off + ((size_t)(u >> 1) * 512 + (u & 1) * 8) * 8);
#pragma unroll
      for (int u = 0; u < 16; ++u) bd |= bad(gh[u], tag);
      if (__all(bd == 0) || spin > SPIN_CAP) break;
      __builtin_amdgcn_s_sleep(2);
    }
    float h0 = 0.f, h1 = 0.f;
#pragma unroll
    for (int u = 0; u < 8; ++u) { h0 += dec(gh[2 * u]); h1 += dec(gh[2 * u + 1]); }
    const float bias = bf2f(b1v);
    h0 = fmaxf(rbf(rbf(h0) + bias), 0.f); h1 = fmaxf(rbf(rbf(h1) + bias), 0.f);
    const unsigned x2 = f2bf(h0 * h0) | (f2bf(h1 * h1) << 16);
    if (lane < 36) *(LAS unsigned*)(L + X2H + (36 * k + lane) * 4) = x2;
  }
  STAMP(6);
  f32x4 oacc = {0.f, 0.f, 0.f, 0.f};
#pragma unroll 1
  for (int grp = 0; grp < 4; ++grp) {
    const unsigned P = P0; P0 = P1; P1 = P2; P2 = P3;
    static_for<0, 4>([&](auto slc) {
      constexpr int sl = decltype(slc)::value;
      const unsigned f = 16 + grp * 4 + sl;
      lds_wait_gt(flags + F_FULL, f);
      const LAS v4u* src = (const LAS v4u*)(ringw + slot * SLOT);
      v4u a0 = src[0], a1 = src[64], a2 = src[128], a3 = src[192];
      asm volatile("" : "+v"(a0), "+v"(a1), "+v"(a2), "+v"(a3) :: "memory");
      flags[F_FREE + k] = f + 1;
      asm volatile("" : "+v"(a0), "+v"(a1), "+v"(a2), "+v"(a3) :: "memory");
      oacc = mfma(a0, make_b(rowbcast<sl * 4 + 0>(P), sel), oacc);
      oacc = mfma(a1, make_b(rowbcast<sl * 4 + 1>(P), sel), oacc);
      oacc = mfma(a2, make_b(rowbcast<sl * 4 + 2>(P), sel), oacc);
      oacc = mfma(a3, make_b(rowbcast<sl * 4 + 3>(P), sel), oacc);
      slot = slot + 1 == NSLOT ? 0 : slot + 1;
    });
  }
  STAMP(7);
  {
#pragma unroll
    for (int i = 0; i < 4; ++i) oacc[i] += dpp<XOR8>(oacc[i]);
    *(LAS f32x4*)(L + TILE_A + k * 1024 + lane * 16) = oacc;
    if (lane == 0) flags[F_LW + k] = __float_as_uint(lsum);
    if (lds_arrive((LAS unsigned*)(L + FLAGS) + F_CO, lane) == 3) {
      f32x4 t = *(LAS f32x4*)(L + TILE_A + lane * 16);
#pragma unroll
      for (int w = 1; w < 4; ++w) t += *(LAS f32x4*)(L + TILE_A + w * 1024 + lane * 16);
      const v4u lw = *(volatile LAS v4u*)(L + FLAGS + F_LW * 4);
      const float lt = (__uint_as_float(lw.x) + __uint_as_float(lw.y)) + (__uint_as_float(lw.z) + __uint_as_float(lw.w));
      const int hi = n >> 3;
      const float v0 = hi ? t[2] : t[0], v1 = hi ? t[3] : t[1];
      const size_t ob = WS_OP + (size_t)cu * 136 * 8;
      gst(p.ws, ob + (32 * r + 16 * hi + (n & 7)) * 8, enc(v0, tag));
      gst(p.ws, ob + (32 * r + 16 * hi + 8 + (n & 7)) * 8, enc(v1, tag));
      if (lane < 2) gst(p.ws, ob + (128 + lane) * 8, enc(lane ? lt : M, tag));
    }
  }
  STAMP(8);
  f32x4 facc = {0.f, 0.f, 0.f, 0.f};
  {
    lds_wait_gt(flags + F_FULL, NFILL);
    const LAS unsigned char* wb = L + W2_OFF + (36 * k + r) * 144 + min(n, 8) * 16;
    const LAS unsigned char* xb = L + X2H + (36 * k + r) * 4;
#pragma unroll
    for (int u = 0; u < 9; ++u) facc = mfma(*(const LAS v4u*)(wb + u * 4 * 144), make_b(*(const LAS unsigned*)(xb + u * 16), sel), facc);
  }
  STAMP(9);
  f32x4 sacc = {0.f, 0.f, 0.f, 0.f};
  {
    const int ob_ = lane >> 5;
    const size_t off = WS_OP + ((size_t)(g * 8 + ob_ * 4) * 136 + 32 * k + (lane & 31)) * 8;
    const size_t offm = WS_OP + ((size_t)(g * 8 + ob_ * 4) * 136 + 128) * 8;
    u64 go[4], gm[4], gl[4];
    for (unsigned spin = 0;; ++spin) {
      unsigned bd = 0;
#pragma unroll
      for (int u = 0; u < 4; ++u) { go[u] = gld(p.ws, off + (size_t)u * 136 * 8); gm[u] = gld(p.ws, offm + (size_t)u * 136 * 8); gl[u] = gld(p.ws, offm + (size_t)u * 136 * 8 + 8); }
#pragma unroll
      for (int u = 0; u < 4; ++u) bd |= bad(go[u], tag) | bad(gm[u], tag) | bad(gl[u], tag);
      if (__all(bd == 0) || spin > SPIN_CAP) break;
      __builtin_amdgcn_s_sleep(1);
    }
    const float Mx = fmaxf(fmaxf(dec(gm[0]), dec(gm[1])), fmaxf(dec(gm[2]), dec(gm[3])));
    float Ls = 0.f, os = 0.f;
#pragma unroll
    for (int u = 0; u < 4; ++u) { const float w = __builtin_amdgcn_exp2f((dec(gm[u]) - Mx) * LOG2E); Ls = fmaf(w, dec(gl[u]), Ls); os = fmaf(w, dec(go[u]), os); }
    const unsigned ob16 = f2bf(os / Ls);
    auto sw = __builtin_amdgcn_permlane32_swap(ob16, ob16, false, false);
    if (lane < 32) *(LAS unsigned*)(L + X2O + (32 * k + lane) * 4) = sw[0] | (sw[1] << 16);
    STAMP(10);
    lds_wait_gt(flags + F_FULL, NFILL + 1);
    const LAS unsigned char* wb = L + WO_OFF + (32 * k + r) * 144 + min(n, 8) * 16;
    const LAS unsigned char* xb = L + X2O + (32 * k + r) * 4;
#pragma unroll
    for (int u = 0; u < 8; ++u) sacc = mfma(*(const LAS v4u*)(wb + u * 4 * 144), make_b(*(const volatile LAS unsigned*)(xb + u * 16), sel), sacc);
  }
  STAMP(11);
  *(LAS f32x4*)(L + TILE_B + k * 2048 + lane * 16) = sacc;
  *(LAS f32x4*)(L + TILE_B + k * 2048 + 1024 + lane * 16) = facc;
  if (lds_arrive((LAS unsigned*)(L + FLAGS) + F_CF, lane) != 3) return;
  {
    f32x4 ts = *(LAS f32x4*)(L + TILE_B + lane * 16), tf = *(LAS f32x4*)(L + TILE_B + 1024 + lane * 16);
#pragma unroll
    for (int w = 1; w < 4; ++w) { ts += *(LAS f32x4*)(L + TILE_B + w * 2048 + lane * 16); tf += *(LAS f32x4*)(L + TILE_B + w * 2048 + 1024 + lane * 16); }
#pragma unroll
    for (int i = 0; i < 4; ++i) if (4 * r + i < 9) {
      const size_t o = WS_FIN + ((((size_t)j * 18 + (n >> 3) * 9 + 4 * r + i) * 32 + g) * 16 + (n & 7)) * 8;
      gst(p.ws, o, enc(ts[i], tag)); gst(p.ws, o + 64, enc(tf[i], tag));
    }
  }
  STAMP(12);
  if (g >= 18) return;
  {
    const size_t off = WS_FIN + ((((size_t)j * 18 + g) * 32 + r) * 16 + n) * 8;
    u64 gf[8];
    for (unsigned spin = 0;; ++spin) {
      unsigned bd = 0;
#pragma unroll
      for (int u = 0; u < 8; ++u) gf[u] = gld(p.ws, off + (size_t)u * 4 * 16 * 8);
#pragma unroll
      for (int u = 0; u < 8; ++u) bd |= bad(gf[u], tag);
      if (__all(bd == 0) || spin > SPIN_CAP) break;
      __builtin_amdgcn_s_sleep(1);
    }
    float t = 0.f;
#pragma unroll
    for (int u = 0; u < 8; ++u) t += dec(gf[u]);
    t = x32_sum(x16_sum(t));
    const float other = dpp<XOR8>(t);
    const float s = rbf(t);
    const float ff = rbf(rbf(other) + bf2f(b2v));
    const float gate = rbf(ff / (1.0f + __expf(-ff)));
    const unsigned o16 = f2bf(bf2f(xres) + rbf(s * gate));
    if (lane < 8) ((GAS u16*)p.out)[rb * D + ocol] = (u16)o16;
    if (bid == 0 && lane == 0) gst(p.ws, WS_E, Eu + 1);
    asm volatile("s_waitcnt vmcnt(0)" ::: "memory");
    if (bid == 0) { while (__builtin_amdgcn_s_memrealtime() - t_start < 6000ull) __builtin_amdgcn_s_sleep(1); }
  }
  STAMP(13);
}

}

extern "C" void kernel_launch(void* const* d_in, const int* in_sizes, int n_in, void* d_out, int out_size, void* d_ws, size_t ws_size, hipStream_t stream) {
  Params p;
  p.x = (const u16*)d_in[0]; p.kc = (const u16*)d_in[1]; p.vc = (const u16*)d_in[2]; p.wq = (const u16*)d_in[3]; p.wo = (const u16*)d_in[4];
  p.w1 = (const u16*)d_in[6]; p.b1 = (const u16*)d_in[7]; p.w2 = (const u16*)d_in[8]; p.b2 = (const u16*)d_in[9];
  p.out = (u16*)d_out; p.ws = (unsigned char*)d_ws;
  hipFuncSetAttribute((const void*)fused_decode_kernel, hipFuncAttributeMaxDynamicSharedMemorySize, LDS_BYTES);
  hipLaunchKernelGGL(fused_decode_kernel, dim3(NWG), dim3(NTHR), LDS_BYTES, stream, p);
}
```

```cpp
#include <hip/hip_runtime.h>
#include <hip/hip_bf16.h>
#include <stdint.h>
#include <type_traits>

namespace {

#define GAS __attribute__((address_space(1)))
#define LAS __attribute__((address_space(3)))
typedef unsigned v4u __attribute__((ext_vector_type(4)));
typedef unsigned v3u __attribute__((ext_vector_type(3)));
typedef float f32x4 __attribute__((ext_vector_type(4)));
typedef __bf16 bf16x2 __attribute__((ext_vector_type(2)));
typedef __bf16 bf16x8 __attribute__((ext_vector_type(8)));
typedef unsigned long long u64;
typedef unsigned short u16;

constexpr int D = 576, H = 32, SK = 4096, DH = 128, F = 4608, HD = H * DH;
constexpr int NWG = 256, NWAVE = 6, NTHR = NWAVE * 64;

#define CODE_BYTES 19984
#define CODE_KB 20
#define TAU 57
#define TAU_F1 8
#define TAU_D1 3
#define TAU_A0 -10
#define BOOST 15
constexpr unsigned Q_PACE = 5, H1_PACE = 9, H2_PACE = 14, ML_PACE = 8;

constexpr int NSLOT = 9, SLOT = 16384;
constexpr int WQ_OFF = 6 * SLOT, W1_OFF = WQ_OFF + 72 * 256;
constexpr int W2_OFF = (32 % NSLOT) * SLOT, WO_OFF = (34 % NSLOT) * SLOT;
static_assert(W1_OFF + 72 * 288 <= NSLOT * SLOT && 32 % NSLOT + 1 < NSLOT && 34 % NSLOT + 1 < NSLOT, "ring map");
constexpr unsigned IT_K = 2, IT_V = 18, IT_W2 = 34, IT_WO = 35;
constexpr int MISC = NSLOT * SLOT;
constexpr int TILE_A = MISC;
constexpr int TILE_B = MISC + 4096;
constexpr int QB = MISC + 8192;
constexpr int X2H = QB + 256;
constexpr int X2X = X2H + 576;
constexpr int WGT = X2X + 288;
constexpr int FSUM = WGT + 32;
constexpr int FLAGS = FSUM + 32;
constexpr int LDS_BYTES = FLAGS + 128;
static_assert(LDS_BYTES <= 163840 && (FLAGS & 15) == 0 && LDS_BYTES > 81920, "LDS");
enum { F_FULL = 0, F_CQ = 1, F_CQR = 3, F_FREE = 4  , F_MW = 8  , F_LW = 12  ,
       F_CM = 16, F_CO = 17, F_CS = 18, F_PW = 19, F_CL = 20, F_WR = 21, F_FR = 22, F_SCHED = 24  , F_T1 = 26, F_OP = 27 };
static_assert(F_FREE * 4 == 16 && F_PW * 4 == 76 && F_SCHED * 4 == 96 && F_T1 * 4 == 104 && F_OP * 4 == 108, "offsets used by the loader");

#define WQ_OFF_V 98304
#define W1_OFF_V 116736
#define FLAGS_V 156832
#define RING_V 147456
static_assert(WQ_OFF_V == WQ_OFF && W1_OFF_V == W1_OFF && FLAGS_V == FLAGS && RING_V == NSLOT * SLOT, "asm constants");

constexpr unsigned WS_E = 0;
constexpr unsigned WS_QP = 4096;
constexpr unsigned WS_HP = WS_QP + NWG * 256 * 8;
constexpr unsigned WS_H2 = WS_HP + NWG * 288 * 8;
constexpr unsigned WS_OP = WS_H2 + NWG * 36 * 8;
constexpr unsigned WS_ML = WS_OP + NWG * 128 * 8;
constexpr unsigned WS_FS = WS_ML + NWG * 2 * 8;
constexpr unsigned WS_FF = WS_FS + 8 * 18 * 32 * 8 * 8;
constexpr unsigned WS_T1 = WS_FF + 8 * 18 * 32 * 8 * 8;
constexpr unsigned WS_END = WS_T1 + 256 * 8;
constexpr unsigned SPIN_CAP = 1u << 15;

struct Params {
  const u16* x; const u16* kc; const u16* vc; const u16* wq; const u16* wo;
  const u16* w1; const u16* b1; const u16* w2; const u16* b2; u16* out; unsigned char* ws;
};
static_assert(sizeof(Params) == 88, "no padding");

__device__ __forceinline__ float bf2f(unsigned b) { return __builtin_bit_cast(float, b << 16); }
__device__ __forceinline__ unsigned f2bf(float f) { return (unsigned)__builtin_bit_cast(u16, __float2bfloat16(f)); }
__device__ __forceinline__ float rbf(float f) { return bf2f(f2bf(f)); }
__device__ __forceinline__ float dot2(unsigned k, unsigned q, float acc) {
  return __builtin_amdgcn_fdot2_f32_bf16(__builtin_bit_cast(bf16x2, k), __builtin_bit_cast(bf16x2, q), acc, false);
}
template <int CTRL> __device__ __forceinline__ unsigned dppu(unsigned x) { return (unsigned)__builtin_amdgcn_mov_dpp((int)x, CTRL, 0xf, 0xf, true); }
template <int CTRL> __device__ __forceinline__ float dpp(float x) { return __uint_as_float(dppu<CTRL>(__float_as_uint(x))); }
template <int I> __device__ __forceinline__ unsigned rowbcast(unsigned x) {
  return (unsigned)__builtin_amdgcn_update_dpp(0, (int)x, 0x150 + I, 0xf, 0xf, true);
}
constexpr int XOR1 = 0xB1, XOR2 = 0x4E, XOR7 = 0x141, XOR8 = 0x128;
__device__ __forceinline__ float sum16(float x) { x += dpp<XOR1>(x); x += dpp<XOR2>(x); x += dpp<XOR7>(x); x += dpp<XOR8>(x); return x; }
__device__ __forceinline__ float max16(float x) { x = fmaxf(x, dpp<XOR1>(x)); x = fmaxf(x, dpp<XOR2>(x)); x = fmaxf(x, dpp<XOR7>(x)); x = fmaxf(x, dpp<XOR8>(x)); return x; }
__device__ __forceinline__ float x16_sum(float x) { auto s = __builtin_amdgcn_permlane16_swap(__float_as_uint(x), __float_as_uint(x), false, false); return __uint_as_float(s[0]) + __uint_as_float(s[1]); }
__device__ __forceinline__ float x32_sum(float x) { auto s = __builtin_amdgcn_permlane32_swap(__float_as_uint(x), __float_as_uint(x), false, false); return __uint_as_float(s[0]) + __uint_as_float(s[1]); }
__device__ __forceinline__ float x16_max(float x) { auto s = __builtin_amdgcn_permlane16_swap(__float_as_uint(x), __float_as_uint(x), false, false); return fmaxf(__uint_as_float(s[0]), __uint_as_float(s[1])); }
__device__ __forceinline__ float x32_max(float x) { auto s = __builtin_amdgcn_permlane32_swap(__float_as_uint(x), __float_as_uint(x), false, false); return fmaxf(__uint_as_float(s[0]), __uint_as_float(s[1])); }
template <int I, int N, class Fn> __device__ __forceinline__ void static_for(Fn&& f) {
  if constexpr (I < N) { f(std::integral_constant<int, I>{}); static_for<I + 1, N>(f); }
}

struct Sel { unsigned s[4]; };
__device__ __forceinline__ Sel make_sel(int lane) {
  const int n = lane & 15;
  const unsigned src = (n & 8) ? 0x0302u : 0x0100u;
  const unsigned one = (n & 1) ? ((src << 16) | 0x0c0cu) : (0x0c0c0000u | src);
  Sel q;
#pragma unroll
  for (int d = 0; d < 4; ++d) q.s[d] = (((n & 7) >> 1) == d) ? one : 0x0c0c0c0cu;
  return q;
}
__device__ __forceinline__ v4u make_b(unsigned p2, const Sel& q) {
  return v4u{__builtin_amdgcn_perm(p2, p2, q.s[0]), __builtin_amdgcn_perm(p2, p2, q.s[1]), __builtin_amdgcn_perm(p2, p2, q.s[2]), __builtin_amdgcn_perm(p2, p2, q.s[3])};
}
__device__ __forceinline__ f32x4 mfma(v4u a, v4u b, f32x4 c) {
  return __builtin_amdgcn_mfma_f32_16x16x32_bf16(__builtin_bit_cast(bf16x8, a), __builtin_bit_cast(bf16x8, b), c, 0, 0, 0);
}

struct Tag { unsigned hi, lo; };
__device__ __forceinline__ u64 enc(float v, const Tag& t) { const unsigned p = (__float_as_uint(v) + 0x80u) >> 8; return ((u64)t.hi << 32) | (u64)(p | t.lo); }
__device__ __forceinline__ unsigned bad(u64 g, const Tag& t) { return ((unsigned)(g >> 32) ^ t.hi) | (((unsigned)g ^ t.lo) >> 24); }
__device__ __forceinline__ float dec(u64 g) { return __uint_as_float((unsigned)g << 8); }
__device__ __forceinline__ u64 gld(const unsigned char* ws, unsigned off) { return __hip_atomic_load((const GAS u64*)(ws + off), __ATOMIC_RELAXED, __HIP_MEMORY_SCOPE_AGENT); }
__device__ __forceinline__ void gst(unsigned char* ws, unsigned off, u64 v) { __hip_atomic_store((GAS u64*)(ws + off), v, __ATOMIC_RELAXED, __HIP_MEMORY_SCOPE_AGENT); }
template <int N, int SLEEP>
__device__ __forceinline__ void gather(const unsigned char* ws, unsigned off, unsigned stride, const Tag& t, u64 (&g)[N]) {
  for (unsigned spin = 0;; ++spin) {
    unsigned bd = 0;
#pragma unroll
    for (int i = 0; i < N; ++i) g[i] = gld(ws, off + i * stride);
#pragma unroll
    for (int i = 0; i < N; ++i) bd |= bad(g[i], t);
    if (__all(bd == 0) || spin > SPIN_CAP) break;
    if (SLEEP) __builtin_amdgcn_s_sleep(SLEEP);
  }
}

__device__ __forceinline__ void lds_wait_gt(volatile LAS unsigned* p, unsigned v) {
  while ((unsigned)__builtin_amdgcn_readfirstlane(*p) <= v) {}
  asm volatile("" ::: "memory");
}
__device__ __forceinline__ unsigned lds_arrive(LAS unsigned* p, int) {
  unsigned old; u64 keep;
  asm volatile("s_waitcnt lgkmcnt(0)\n\ts_mov_b64 %1, exec\n\ts_mov_b64 exec, 1\n\tds_add_rtn_u32 %0, %2, %3\n\ts_mov_b64 exec, %1\n\ts_waitcnt lgkmcnt(0)"
               : "=&v"(old), "=&s"(keep) : "v"((unsigned)(uintptr_t)p), "v"(1u) : "memory");
  return __builtin_amdgcn_readfirstlane(old);
}
template <int STRIDE>
__device__ __forceinline__ void gld4(const unsigned char* ws, unsigned off, u64 (&g)[4]) {
  asm volatile("global_load_dwordx2 %0, %4, %5 sc1\n\tglobal_load_dwordx2 %1, %4, %5 offset:%6 sc1\n\t"
               "global_load_dwordx2 %2, %4, %5 offset:%7 sc1\n\tglobal_load_dwordx2 %3, %4, %5 offset:%8 sc1\n\ts_waitcnt vmcnt(0)"
               : "=&v"(g[0]), "=&v"(g[1]), "=&v"(g[2]), "=&v"(g[3]) : "v"(off), "s"(ws), "n"(STRIDE), "n"(2 * STRIDE), "n"(3 * STRIDE) : "memory");
}
template <int STRIDE>
__device__ __forceinline__ void gather4(const unsigned char* ws, unsigned off, const Tag& t, u64 (&g)[4]) {
  for (unsigned spin = 0;; ++spin) {
    gld4<STRIDE>(ws, off, g);
    const unsigned bd = bad(g[0], t) | bad(g[1], t) | bad(g[2], t) | bad(g[3], t);
    if (__all(bd == 0) || spin > SPIN_CAP) break;
  }
}

#define NTS " nt"

#define STR2(x) #x
#define STR(x) STR2(x)
#define WAITFREE(T) \
  "LWF" T "%=:\n\t" \
  "ds_read_b128 v[104:107], v101 offset:16\n\t" \
  "s_waitcnt lgkmcnt(0)\n\t" \
  "v_min_u32 v104, v104, v105\n\t" \
  "v_min3_u32 v104, v104, v106, v107\n\t" \
  "s_nop 0\n\t" \
  "v_readfirstlane_b32 s66, v104\n\t" \
  "s_cmp_ge_u32 s66, s63\n\t" \
  "s_cbranch_scc1 LWD" T "%=\n\t" \
  "s_sleep 1\n\t" \
  "s_branch LWF" T "%=\n\t" \
  "LWD" T "%=:\n\t"
#define PUBLISH(R) "v_mov_b32 v104, " R "\n\tds_write_b32 v101, v104\n\t"

__global__ void __launch_bounds__(NTHR, 2) fused_decode_kernel(Params p) {
  extern __shared__ __attribute__((aligned(16))) unsigned char smem[];
  LAS unsigned char* L = (LAS unsigned char*)smem;
  asm volatile(
      "v_readfirstlane_b32 s60, %[tid]\n\t"
      "s_cmp_lg_u32 s60, 0\n\t"
      "s_cbranch_scc1 LSKIP%=\n\t"
      "s_load_dwordx4 s[44:47], %[ka], 0x8\n\t"
      "s_load_dwordx4 s[48:51], %[ka], 0x18\n\t"
      "s_load_dwordx2 s[52:53], %[ka], 0x28\n\t"
      "s_load_dwordx2 s[54:55], %[ka], 0x38\n\t"
      "s_load_dwordx2 s[56:57], %[ka], 0x50\n\t"
      "s_load_dwordx2 s[98:99], %[ka], 0x0\n\t"
      "s_setprio 2\n\t"
      "s_mov_b32 s36, %[lb]\n\t"
      "s_getpc_b64 s[64:65]\n\t"
      "s_lshr_b32 s61, %[bid], 3\n\t"
      "s_and_b32 s64, s64, 0xffffff00\n\t"
      "s_cmp_ge_u32 s61, " STR(CODE_KB) "\n\t"
      "s_cselect_b32 s62, " STR(CODE_KB) ", 0\n\t"
      "s_sub_u32 s61, s61, s62\n\t"
      "s_lshl_b32 s61, s61, 10\n\t"
      "v_lshlrev_b32 v100, 4, %[tid]\n\t"
      "s_add_u32 m0, s36, 0x14000\n\t"
      "v_add_u32 v102, s61, v100\n\t"
      "v_min_u32 v102, " STR(CODE_BYTES) " - 16, v102\n\t"
      "global_load_lds_dwordx4 v102, s[64:65]\n\t"
      "v_mov_b32 v101, s36\n\t"
      "v_add_u32 v101, " STR(FLAGS_V) ", v101\n\t"
      "v_lshl_add_u32 v103, %[tid], 2, v101\n\t"
      "v_mov_b32 v104, 0\n\t"
      "s_mov_b32 exec_hi, 0\n\t"
      "ds_write_b32 v103, v104\n\t"
      "s_mov_b32 exec_hi, -1\n\t"
      "s_and_b32 s60, %[bid], 7\n\t"
      "s_lshl_b32 s60, s60, 2\n\t"
      "s_lshr_b32 s61, %[bid], 6\n\t"
      "s_add_u32 s60, s60, s61\n\t"
      "s_bfe_u32 s61, %[bid], 0x30003\n\t"
      "s_mul_i32 s39, s61, 589824\n\t"
      "s_lshl_b32 s62, s60, 8\n\t"
      "s_add_u32 s39, s39, s62\n\t"
      "s_mul_i32 s40, s61, 663552\n\t"
      "s_mul_i32 s62, s60, 288\n\t"
      "s_add_u32 s40, s40, s62\n\t"
      "s_lshr_b32 s62, s61, 2\n\t"
      "s_lshl_b32 s62, s62, 5\n\t"
      "s_add_u32 s62, s62, s60\n\t"
      "s_lshl_b32 s38, s62, 20\n\t"
      "s_and_b32 s62, s61, 3\n\t"
      "s_lshl_b32 s62, s62, 18\n\t"
      "s_add_u32 s38, s38, s62\n\t"
      "s_mul_i32 s63, s61, 144\n\t"
      "s_mul_i32 s41, s60, 165888\n\t"
      "s_add_u32 s41, s41, s63\n\t"
      "s_mul_i32 s42, s60, 147456\n\t"
      "s_add_u32 s42, s42, s63\n\t"
      "s_waitcnt lgkmcnt(0)\n\t"
      "s_and_b32 s62, s61, 3\n\t"
      "s_and_b32 s66, s60, 3\n\t"
      "s_cmp_eq_u32 s62, 0\n\t"
      "s_cbranch_scc1 LNT%=\n\t"
      "s_mov_b64 s[64:65], s[44:45]\n\t"
      "s_mov_b32 s63, s38\n\t"
      "s_cmp_eq_u32 s62, 2\n\t"
      "s_cselect_b64 s[64:65], s[46:47], s[64:65]\n\t"
      "s_cmp_eq_u32 s62, 3\n\t"
      "s_cbranch_scc0 LDT%=\n\t"
      "s_cmp_eq_u32 s61, 7\n\t"
      "s_cbranch_scc0 LT3%=\n\t"
      "s_cmp_eq_u32 s66, 3\n\t"
      "s_cbranch_scc0 LNT%=\n\t"
      "s_mov_b64 s[64:65], s[98:99]\n\t"
      "s_mov_b32 s63, 0\n\t"
      "s_branch LDT%=\n\t"
      "LT3%=:\n\t"
      "s_mul_i32 s63, s66, 0x240000\n\t"
      "s_mov_b64 s[64:65], s[52:53]\n\t"
      "s_cmp_eq_u32 s66, 3\n\t"
      "s_cselect_b64 s[64:65], s[56:57], s[64:65]\n\t"
      "s_cselect_b32 s63, 0, s63\n\t"
      "LDT%=:\n\t"
      "s_add_u32 m0, s36, 0x14400\n\t"
      "v_mov_b32 v105, s63\n\t"
      "s_mov_b64 exec, 1\n\t"
      "global_load_lds_dword v105, s[64:65]\n\t"
      "s_mov_b64 exec, -1\n\t"
      "LNT%=:\n\t"
      "s_add_u32 m0, s36, " STR(WQ_OFF_V) "\n\t"
      "v_lshrrev_b32 v102, 4, %[tid]\n\t"
      "v_and_b32 v103, 15, %[tid]\n\t"
      "v_lshlrev_b32 v103, 4, v103\n\t"
      "v_lshl_or_b32 v102, v102, 13, v103\n\t"
      "v_add_u32 v102, s39, v102\n\t"
      "s_movk_i32 s60, 18\n\t"
      "LWQ%=:\n\t"
      "global_load_lds_dwordx4 v102, s[48:49]" NTS "\n\t"
      "s_add_u32 m0, m0, 0x400\n\t"
      "v_add_u32 v102, 0x8000, v102\n\t"
      "s_sub_u32 s60, s60, 1\n\t"
      "s_cmp_lg_u32 s60, 0\n\t"
      "s_cbranch_scc1 LWQ%=\n\t"
      "s_waitcnt lgkmcnt(0)\n\t"
      "s_barrier\n\t"
      "v_lshlrev_b32 v100, 4, %[tid]\n\t"
      "v_add_u32 v100, s38, v100\n\t"
      "v_mul_u32_u24 v103, 57, %[tid]\n\t"
      "v_lshrrev_b32 v103, 10, v103\n\t"
      "v_mul_u32_u24 v104, 18, v103\n\t"
      "v_sub_u32 v104, %[tid], v104\n\t"
      "v_lshlrev_b32 v104, 4, v104\n\t"
      "v_mul_u32_u24 v103, 0x2400, v103\n\t"
      "s_add_u32 m0, s36, " STR(W1_OFF_V) "\n\t"
      "v_add_u32 v102, v103, v104\n\t"
      "v_add_u32 v102, s40, v102\n\t"
      "s_mov_b32 exec_hi, 0x003fffff\n\t"
      "s_movk_i32 s60, 24\n\t"
      "LW1%=:\n\t"
      "global_load_lds_dwordx4 v102, s[52:53]\n\t"
      "s_add_u32 m0, m0, 864\n\t"
      "v_add_u32 v102, 0x6c00, v102\n\t"
      "s_sub_u32 s60, s60, 1\n\t"
      "s_cmp_lg_u32 s60, 0\n\t"
      "s_cbranch_scc1 LW1%=\n\t"
      "s_mov_b32 exec_hi, -1\n\t"
      "s_waitcnt vmcnt(24)\n\t"
      PUBLISH("1")
      "v_mul_u32_u24 v103, 57, %[tid]\n\t"
      "v_lshrrev_b32 v103, 9, v103\n\t"
      "v_mul_u32_u24 v104, 9, v103\n\t"
      "v_sub_u32 v104, %[tid], v104\n\t"
      "v_lshlrev_b32 v104, 4, v104\n\t"
      "v_mul_u32_u24 v103, 0x480, v103\n\t"
      "v_add_u32 v108, v103, v104\n\t"
      "s_mov_b32 s61, 0\n\t"
      "s_mov_b32 s62, s36\n\t"
      "s_add_u32 s67, s36, " STR(RING_V) "\n\t"
      "LF%=:\n\t"
      "s_cmp_lg_u32 s61, 6\n\t"
      "s_cbranch_scc1 LNOPW%=\n\t"
      "LPW%=:\n\t"
      "ds_read_b32 v104, v101 offset:76\n\t"
      "s_waitcnt lgkmcnt(0)\n\t"
      "v_readfirstlane_b32 s66, v104\n\t"
      "s_cmp_ge_u32 s66, 5\n\t"
      "s_cbranch_scc1 LNOPW%=\n\t"
      "s_sleep 1\n\t"
      "s_branch LPW%=\n\t"
      "LNOPW%=:\n\t"
      "s_sub_u32 s63, s61, 8\n\t"
      "s_mul_i32 s96, s61, " STR(TAU) "\n\t"
      "s_min_u32 s97, s61, " STR(TAU_F1) "\n\t"
      "s_mul_i32 s97, s97, " STR(TAU_D1) "\n\t"
      "s_add_u32 s96, s96, s97\n\t"
      "LTOP%=:\n\t"
      "ds_read_b128 v[104:107], v101 offset:16\n\t"
      "ds_read_b64 v[110:111], v101 offset:96\n\t"
      "s_memrealtime s[92:93]\n\t"
      "s_waitcnt lgkmcnt(0)\n\t"
      "v_min_u32 v104, v104, v105\n\t"
      "v_min3_u32 v104, v104, v106, v107\n\t"
      "v_readfirstlane_b32 s94, v110\n\t"
      "v_readfirstlane_b32 s95, v111\n\t"
      "v_readfirstlane_b32 s66, v104\n\t"
      "s_cmp_ge_i32 s66, s63\n\t"
      "s_cbranch_scc0 LSLP%=\n\t"
      "s_mov_b32 s91, 0\n\t"
      "s_cmp_eq_u32 s95, 0\n\t"
      "s_cbranch_scc1 LGO%=\n\t"
      "s_add_u32 s97, s96, s94\n\t"
      "s_sub_u32 s91, s92, s97\n\t"
      "s_cmp_ge_i32 s91, 0\n\t"
      "s_cbranch_scc1 LGO%=\n\t"
      "LSLP%=:\n\t"
      "s_sleep 1\n\t"
      "s_branch LTOP%=\n\t"
      "LGO%=:\n\t"
      "s_cmp_lg_u32 s61, 1\n\t"
      "s_cbranch_scc1 LNT1%=\n\t"
      "s_or_b32 s97, s92, 1\n\t"
      "v_mov_b32 v104, s97\n\t"
      "ds_write_b32 v101, v104 offset:104\n\t"
      "LNT1%=:\n\t"
      "s_cmp_lt_u32 s61, 16\n\t"
      "s_cselect_b64 s[64:65], s[44:45], s[46:47]\n\t"
      "s_and_b32 s63, s61, 15\n\t"
      "s_lshl_b32 s63, s63, 14\n\t"
      "s_mov_b32 m0, s62\n\t"
      "v_add_u32 v102, s63, v100\n\t"
      ".rept 4\n\t"
      "global_load_lds_dwordx4 v102, s[64:65]" NTS "\n\t"
      "global_load_lds_dwordx4 v102, s[64:65] offset:1024" NTS "\n\t"
      "global_load_lds_dwordx4 v102, s[64:65] offset:2048" NTS "\n\t"
      "global_load_lds_dwordx4 v102, s[64:65] offset:3072" NTS "\n\t"
      "s_add_u32 m0, m0, 0x1000\n\t"
      "v_add_u32 v102, 0x1000, v102\n\t"
      ".endr\n\t"
      "s_add_u32 s62, s62, 0x4000\n\t"
      "s_cmp_eq_u32 s62, s67\n\t"
      "s_cselect_b32 s62, s36, s62\n\t"
      "s_add_u32 s63, s61, 2\n\t"
      "s_cmp_lt_i32 s91, " STR(BOOST) "\n\t"
      "s_cbranch_scc1 LNB%=\n\t"
      "s_cmp_eq_u32 s61, 0\n\t"
      "s_cbranch_scc1 LNB%=\n\t"
      "s_cmp_eq_u32 s61, 31\n\t"
      "s_cbranch_scc1 LNB%=\n\t"
      "s_sub_u32 s63, s63, 1\n\t"
      "s_waitcnt vmcnt(32)\n\t"
      PUBLISH("s63")
      "s_branch LBD%=\n\t"
      "LNB%=:\n\t"
      "s_waitcnt vmcnt(16)\n\t"
      PUBLISH("s63")
      "LBD%=:\n\t"
      "s_add_u32 s61, s61, 1\n\t"
      "s_cmp_lt_u32 s61, 32\n\t"
      "s_cbranch_scc1 LF%=\n\t"
      "s_movk_i32 s63, 27\n\t"
      WAITFREE("C")
      "s_mov_b32 m0, s62\n\t"
      "v_add_u32 v102, s41, v108\n\t"
      "s_mov_b32 exec_hi, 0x7fffffff\n\t"
      "s_movk_i32 s60, 20\n\t"
      "LW2%=:\n\t"
      "global_load_lds_dwordx4 v102, s[54:55]\n\t"
      "s_add_u32 m0, m0, 1008\n\t"
      "v_add_u32 v102, 0x1f80, v102\n\t"
      "s_sub_u32 s60, s60, 1\n\t"
      "s_cmp_lg_u32 s60, 0\n\t"
      "s_cbranch_scc1 LW2%=\n\t"
      "s_mov_b32 exec_hi, 0xf\n\t"
      "global_load_lds_dwordx4 v102, s[54:55]\n\t"
      "s_mov_b32 exec_hi, -1\n\t"
      "s_waitcnt vmcnt(21)\n\t"
      PUBLISH("34")
      "LOP%=:\n\t"
      "ds_read_b32 v104, v101 offset:108\n\t"
      "s_waitcnt lgkmcnt(0)\n\t"
      "v_readfirstlane_b32 s66, v104\n\t"
      "s_cmp_lg_u32 s66, 0\n\t"
      "s_cbranch_scc0 LOP%=\n\t"
      "s_add_u32 m0, s62, 0x8000\n\t"
      "v_add_u32 v102, s42, v108\n\t"
      "s_mov_b32 exec_hi, 0x7fffffff\n\t"
      "s_movk_i32 s60, 18\n\t"
      "LWO%=:\n\t"
      "global_load_lds_dwordx4 v102, s[50:51]\n\t"
      "s_add_u32 m0, m0, 1008\n\t"
      "v_add_u32 v102, 0x1f80, v102\n\t"
      "s_sub_u32 s60, s60, 1\n\t"
      "s_cmp_lg_u32 s60, 0\n\t"
      "s_cbranch_scc1 LWO%=\n\t"
      "s_mov_b32 exec_lo, 0x3ffff\n\t"
      "s_mov_b32 exec_hi, 0\n\t"
      "global_load_lds_dwordx4 v102, s[50:51]\n\t"
      "s_mov_b64 exec, -1\n\t"
      "s_waitcnt vmcnt(19)\n\t"
      PUBLISH("35")
      "s_waitcnt vmcnt(0)\n\t"
      PUBLISH("36")
      "s_waitcnt lgkmcnt(0)\n\t"
      "s_endpgm\n\t"
      "LSKIP%=:"
      :
      : [tid] "v"(threadIdx.x), [bid] "s"(blockIdx.x), [lb] "s"((unsigned)(uintptr_t)L), [ka] "s"(__builtin_amdgcn_kernarg_segment_ptr())
      : "memory", "scc", "vcc", "s36", "s38", "s39", "s40", "s41", "s42", "s44", "s45", "s46", "s47", "s48", "s49", "s50", "s51", "s52", "s53", "s54", "s55",
        "s60", "s61", "s62", "s63", "s64", "s65", "s66", "s67", "s56", "s57", "s91", "s92", "s93", "s94", "s95", "s96", "s97", "s98", "s99",
        "v100", "v101", "v102", "v103", "v104", "v105", "v106", "v107", "v108", "v110", "v111");

  volatile LAS unsigned* flags = (volatile LAS unsigned*)(L + FLAGS);
  const int tid = threadIdx.x, lane = tid & 63, wave = __builtin_amdgcn_readfirstlane(tid >> 6);
  const int bid = blockIdx.x;
  const int g = (bid & 7) * 4 + (bid >> 6), j = (bid >> 3) & 7;
  const int bb = j >> 2;
  const int cu = g * 8 + j;
  const int k = wave - 1;
  const int n = lane & 15, r = lane >> 4;
  const int row0 = 72 * j + 18 * min(k, 3);
  const int rho = min(g, 17), rb = rho / 9, ri = rho - rb * 9;
  const int ocol = 72 * j + 8 * ri + (lane & 7);
  __builtin_amdgcn_sched_barrier(0);
  __builtin_amdgcn_s_barrier();
  asm volatile("" ::: "memory");
  __builtin_amdgcn_sched_barrier(0);
  unsigned xs[9], xv[5];
  {
    const GAS unsigned* xd = (const GAS unsigned*)p.x + (lane >> 5) * (D / 2) + (lane & 31) * 3;
#pragma unroll
    for (int t = 0; t < 3; ++t) { const v3u v = *(const GAS v3u*)(xd + 96 * t); xs[3 * t] = v.x; xs[3 * t + 1] = v.y; xs[3 * t + 2] = v.z; }
#pragma unroll
    for (int u = 0; u < 5; ++u) xv[u] = ((const GAS u16*)p.x)[(n >> 3) * D + row0 + min(4 * u + r, 17)];
  }
  const u64 E = gld(p.ws, WS_E);
  const unsigned xres = ((const GAS u16*)p.x)[rb * D + ocol];
  const unsigned b2v = ((const GAS u16*)p.b2)[ocol];
  __builtin_amdgcn_sched_barrier(0);
  const Sel sel = make_sel(lane);
  float mb, den, mb0, mb1, den0, den1;
  {
    float s = 0.f;
#pragma unroll
    for (int t = 0; t < 9; ++t) s += bf2f(xs[t] & 0xffffu) + __uint_as_float(xs[t] & 0xffff0000u);
    s = x16_sum(sum16(s));
    const float mean = s * (1.0f / D);
    float ss = 0.f;
#pragma unroll
    for (int t = 0; t < 9; ++t) { const float a = bf2f(xs[t] & 0xffffu) - mean, b = __uint_as_float(xs[t] & 0xffff0000u) - mean; ss = fmaf(a, a, ss); ss = fmaf(b, b, ss); }
    ss = x16_sum(sum16(ss));
    const float m_ = rbf(mean), d_ = rbf(sqrtf(rbf(rbf(ss * (1.0f / D)) + 1e-5f)));
    auto sm = __builtin_amdgcn_permlane32_swap(__float_as_uint(m_), __float_as_uint(m_), false, false);
    auto sd = __builtin_amdgcn_permlane32_swap(__float_as_uint(d_), __float_as_uint(d_), false, false);
    mb0 = __uint_as_float(sm[0]); mb1 = __uint_as_float(sm[1]); den0 = __uint_as_float(sd[0]); den1 = __uint_as_float(sd[1]);
    mb = (n & 8) ? mb1 : mb0;
    den = (n & 8) ? den1 : den0;
  }
  auto xnorm = [&](unsigned raw) { return f2bf(rbf(bf2f(raw) - mb) / den); };
  Tag tag;
  const unsigned Elo = __builtin_amdgcn_readfirstlane((unsigned)E), Ehi = __builtin_amdgcn_readfirstlane((unsigned)(E >> 32));
  const u64 Eu = ((u64)Ehi << 32) | Elo;
  {
    u64 z = Eu + 0x9E3779B97F4A7C15ull;
    z = (z ^ (z >> 30)) * 0xBF58476D1CE4E5B9ull;
    z = (z ^ (z >> 27)) * 0x94D049BB133111EBull;
    z ^= z >> 31;
    tag.hi = (unsigned)(z >> 32); tag.lo = (unsigned)z & 0xff000000u;
  }
  constexpr float LOG2E = 1.4426950408889634f;

  if (wave == 5) {
    {
      const int l2 = min(lane, 35);
      const unsigned xa = ((const GAS unsigned*)p.x)[36 * j + l2], xb = ((const GAS unsigned*)p.x)[D / 2 + 36 * j + l2];
      const GAS unsigned char* tp = j == 2 ? (const GAS unsigned char*)p.w2 + (size_t)g * 144 * D * 2 : j == 3 ? (const GAS unsigned char*)p.wo + (size_t)g * 128 * D * 2
                                  : j == 4 ? (const GAS unsigned char*)p.out : (const GAS unsigned char*)p.x;
      const unsigned t2 = *(const GAS unsigned*)tp;
      const unsigned e0 = f2bf(rbf(bf2f(xa & 0xffffu) - mb0) / den0) | (f2bf(rbf(bf2f(xb & 0xffffu) - mb1) / den1) << 16);
      const unsigned e1 = f2bf(rbf(bf2f(xa >> 16) - mb0) / den0) | (f2bf(rbf(bf2f(xb >> 16) - mb1) / den1) << 16);
      if (lane < 36) { *(LAS unsigned*)(L + X2X + lane * 8) = e0; *(LAS unsigned*)(L + X2X + lane * 8 + 4) = e1; }
      asm volatile("" :: "v"(t2));
    }
    unsigned t1;
    {
      f32x4 a0 = {0.f, 0.f, 0.f, 0.f}, a1 = {0.f, 0.f, 0.f, 0.f};
      lds_wait_gt(flags + F_FULL, 1);
      lds_wait_gt(flags + F_T1, 0);
      t1 = __builtin_amdgcn_readfirstlane(flags[F_T1]);
      if (lane == 0) gst(p.ws, WS_T1 + cu * 8, ((u64)tag.hi << 32) | tag.lo | (t1 & 0xffffffu));
      const LAS unsigned char* wb = L + W1_OFF + r * 288 + min(n, 8) * 16;
#pragma unroll 1
      for (int q = 0; q < 3; ++q) {
        v4u w0[6], w1[6]; unsigned xr[6];
#pragma unroll
        for (int u = 0; u < 6; ++u) {
          w0[u] = *(const LAS v4u*)(wb + (q * 6 + u) * 4 * 288); w1[u] = *(const LAS v4u*)(wb + (q * 6 + u) * 4 * 288 + 144);
          xr[u] = *(const LAS unsigned*)(L + X2X + ((q * 6 + u) * 4 + r) * 4);
        }
        __builtin_amdgcn_sched_barrier(0);
#pragma unroll
        for (int u = 0; u < 6; ++u) { const v4u b = make_b(xr[u], sel); a0 = mfma(w0[u], b, a0); a1 = mfma(w1[u], b, a1); }
      }
      asm volatile("" : "+v"(a0), "+v"(a1) :: "memory");
      lds_arrive((LAS unsigned*)(L + FLAGS) + F_PW, lane);
      const unsigned ho = WS_HP + (cu * 288 + (8 * 4 * r + (n & 7)) * 2 + (n >> 3)) * 8;
      if (lane < 48) { gst(p.ws, ho, enc(a0[0], tag)); gst(p.ws, ho + 72 * 16, enc(a1[0], tag)); }
      if (lane < 32) {
#pragma unroll
        for (int i = 1; i < 4; ++i) { gst(p.ws, ho + i * 128, enc(a0[i], tag)); gst(p.ws, ho + 72 * 16 + i * 128, enc(a1[i], tag)); }
      }
    }
    {
      const int l2 = min(lane, 35);
      const unsigned bias = ((const GAS u16*)p.b1)[g * 144 + 18 * j + (l2 >> 1)];
      {
        u64 gt[1];
        gather<1, 8>(p.ws, WS_T1 + (g * 8 + (lane & 7)) * 8, 0, tag, gt);
        const int d = ((int)(((unsigned)gt[0] - t1) << 8)) >> 8;
        float ds = (float)d; ds += dpp<XOR1>(ds); ds += dpp<XOR2>(ds); ds += dpp<XOR7>(ds);
        const unsigned base = t1 + (unsigned)(int)(ds * 0.125f) + (unsigned)(TAU_A0 - TAU - TAU_D1);
        if (lane == 0) *(volatile LAS u64*)(L + FLAGS + F_SCHED * 4) = (1ull << 32) | base;
      }
      lds_wait_gt(flags + F_FULL, H1_PACE);
      u64 gh[8];
      gather<8, 16>(p.ws, WS_HP + (g * 8 * 288 + 36 * j + l2) * 8, 288 * 8, tag, gh);
      float h = 0.f;
#pragma unroll
      for (int u = 0; u < 8; ++u) h += dec(gh[u]);
      h = fmaxf(rbf(rbf(h) + bf2f(bias)), 0.f);
      if (lane < 36) gst(p.ws, WS_H2 + (cu * 36 + lane) * 8, enc(rbf(h * h), tag));
      lds_wait_gt(flags + F_FULL, H2_PACE);
      u64 g2[5];
      for (unsigned spin = 0;; ++spin) {
        unsigned bd = 0;
#pragma unroll
        for (int c = 0; c < 5; ++c) g2[c] = gld(p.ws, WS_H2 + (g * 288 + min(64 * c + lane, 287)) * 8);
#pragma unroll
        for (int c = 0; c < 5; ++c) bd |= bad(g2[c], tag);
        if (__all(bd == 0) || spin > SPIN_CAP) break;
        __builtin_amdgcn_s_sleep(16);
      }
#pragma unroll
      for (int c = 0; c < 5; ++c) {
        const unsigned v = __float_as_uint(dec(g2[c])) >> 16, o = dppu<XOR1>(v);
        if (!(lane & 1) && 64 * c + lane < 288) *(LAS unsigned*)(L + X2H + (32 * c + (lane >> 1)) * 4) = v | (o << 16);
      }
    }
    {
      lds_wait_gt(flags + F_FULL, IT_V + ML_PACE);
      u64 gm[1];
      gather<1, 16>(p.ws, WS_ML + (g * 16 + n) * 8, 0, tag, gm);
      const unsigned v = __float_as_uint(dec(gm[0]));
      float ms[4], ls[4];
      static_for<0, 4>([&](auto sc) { constexpr int s = decltype(sc)::value;
        const unsigned m0 = rowbcast<2 * s>(v), m1 = rowbcast<8 + 2 * s>(v), l0 = rowbcast<2 * s + 1>(v), l1 = rowbcast<8 + 2 * s + 1>(v);
        ms[s] = __uint_as_float((n & 8) ? m1 : m0); ls[s] = __uint_as_float((n & 8) ? l1 : l0); });
      const float Mx = fmaxf(fmaxf(ms[0], ms[1]), fmaxf(ms[2], ms[3]));
      float w[4], Ls = 0.f;
#pragma unroll
      for (int s = 0; s < 4; ++s) { w[s] = __builtin_amdgcn_exp2f((ms[s] - Mx) * LOG2E); Ls = fmaf(w[s], ls[s], Ls); }
      if ((lane & 55) == 0) *(LAS f32x4*)(L + WGT + (n >> 3) * 16) = f32x4{w[0] / Ls, w[1] / Ls, w[2] / Ls, w[3] / Ls};
      asm volatile("s_waitcnt lgkmcnt(0)" ::: "memory");
      flags[F_WR] = 1u;
    }
    {
      f32x4 facc = {0.f, 0.f, 0.f, 0.f};
      unsigned xh[36];
#pragma unroll
      for (int u = 0; u < 36; ++u) xh[u] = *(const LAS unsigned*)(L + X2H + (4 * u + r) * 4);
      __builtin_amdgcn_sched_barrier(0);
      lds_wait_gt(flags + F_FULL, IT_W2);
      const LAS unsigned char* wb = L + W2_OFF + r * 144 + min(n, 8) * 16;
      f32x4 facc1 = {0.f, 0.f, 0.f, 0.f};
#pragma unroll 1
      for (int q = 0; q < 3; ++q) {
        v4u w[12];
#pragma unroll
        for (int u = 0; u < 12; ++u) w[u] = *(const LAS v4u*)(wb + (q * 12 + u) * 4 * 144);
        __builtin_amdgcn_sched_barrier(0);
#pragma unroll
        for (int u = 0; u < 12; u += 2) { facc = mfma(w[u], make_b(xh[u], sel), facc); facc1 = mfma(w[u + 1], make_b(xh[u + 1], sel), facc1); }
#pragma unroll
        for (int u = 0; u < 24; ++u) xh[u] = xh[u + 12];
      }
      facc += facc1;
      asm volatile("" : "+v"(facc) :: "memory");
      const unsigned fo = WS_FF + (((j * 18 + (n >> 3) * 9 + 4 * r) * 32 + g) * 8 + (n & 7)) * 8;
      if (lane < 48) gst(p.ws, fo, enc(facc[0], tag));
      if (lane < 32) {
#pragma unroll
        for (int i = 1; i < 4; ++i) gst(p.ws, fo + i * 2048, enc(facc[i], tag));
      }
    }
    if (g < 18) {
      u64 gf[4];
      gather4<8 * 8 * 8>(p.ws, WS_FF + (((j * 18 + g) * 32 + (lane >> 3)) * 8 + (lane & 7)) * 8, tag, gf);
      float t = (dec(gf[0]) + dec(gf[1])) + (dec(gf[2]) + dec(gf[3]));
      t += dpp<XOR8>(t);
      t = x32_sum(x16_sum(t));
      const float ff = rbf(rbf(t) + bf2f(b2v));
      const float gate = rbf(ff / (1.0f + __expf(-ff)));
      if (lane < 8) *(LAS float*)(L + FSUM + lane * 4) = gate;
      asm volatile("s_waitcnt lgkmcnt(0)" ::: "memory");
      flags[F_FR] = 1u;
    }
    return;
  }

  {
    v4u bx[5];
#pragma unroll
    for (int u = 0; u < 5; ++u) bx[u] = make_b((4 * u + r < 18) ? xnorm(xv[u]) * 0x10001u : 0u, sel);
    f32x4 acc = {0.f, 0.f, 0.f, 0.f};
    lds_wait_gt(flags + F_FULL, 0);
#pragma unroll
    for (int u = 0; u < 5; ++u) acc = mfma(*(const LAS v4u*)(L + WQ_OFF + (18 * k + min(4 * u + r, 17)) * 256 + n * 16), bx[u], acc);
    *(LAS f32x4*)(L + TILE_A + k * 1024 + lane * 16) = acc;
    lds_arrive((LAS unsigned*)(L + FLAGS) + F_PW, lane);
    if (lds_arrive((LAS unsigned*)(L + FLAGS) + F_CQ, lane) == 3) {
      f32x4 t = *(LAS f32x4*)(L + TILE_A + lane * 16);
#pragma unroll
      for (int w = 1; w < 4; ++w) t += *(LAS f32x4*)(L + TILE_A + w * 1024 + lane * 16);
#pragma unroll
      for (int i = 0; i < 4; ++i) gst(p.ws, WS_QP + (cu * 256 + i * 64 + lane) * 8, enc(t[i], tag));
    }
  }
  v4u qv;
  {
    u64 gq[4];
    lds_wait_gt(flags + F_FULL, Q_PACE);
    gather<4, 8>(p.ws, WS_QP + ((g * 8 + (lane >> 5)) * 256 + ((lane >> 3) & 3) * 64 + k * 16 + bb * 8 + (lane & 7)) * 8, 2 * 256 * 8, tag, gq);
    const float q32 = x32_sum((dec(gq[0]) + dec(gq[1])) + (dec(gq[2]) + dec(gq[3])));
    if (lane < 32) *(LAS u16*)(L + QB + (32 * k + lane) * 2) = (u16)f2bf(q32);
    lds_arrive((LAS unsigned*)(L + FLAGS) + F_CQR, lane);
    lds_wait_gt(flags + F_CQR, 3);
    qv = *(LAS v4u*)(L + QB + n * 16);
  }
  float sc0 = 0.f, sc1 = 0.f, sc2 = 0.f, sc3 = 0.f;
  int slot = 0;
  const LAS unsigned char* ringw = L + k * 4096 + lane * 16;
#pragma unroll 1
  for (int grp = 0; grp < 4; ++grp) {
    float xx[16];
#pragma unroll
    for (int sl = 0; sl < 4; ++sl) {
      const unsigned f = grp * 4 + sl;
      lds_wait_gt(flags + F_FULL, IT_K + f);
      const LAS v4u* src = (const LAS v4u*)(ringw + slot * SLOT);
      v4u a0 = src[0], a1 = src[64], a2 = src[128], a3 = src[192];
      asm volatile("" : "+v"(a0), "+v"(a1), "+v"(a2), "+v"(a3) :: "memory");
      flags[F_FREE + k] = f + 1;
      asm volatile("" : "+v"(a0), "+v"(a1), "+v"(a2), "+v"(a3) :: "memory");
      xx[sl * 4 + 0] = dot2(a0.w, qv.w, dot2(a0.z, qv.z, dot2(a0.y, qv.y, dot2(a0.x, qv.x, 0.f))));
      xx[sl * 4 + 1] = dot2(a1.w, qv.w, dot2(a1.z, qv.z, dot2(a1.y, qv.y, dot2(a1.x, qv.x, 0.f))));
      xx[sl * 4 + 2] = dot2(a2.w, qv.w, dot2(a2.z, qv.z, dot2(a2.y, qv.y, dot2(a2.x, qv.x, 0.f))));
      xx[sl * 4 + 3] = dot2(a3.w, qv.w, dot2(a3.z, qv.z, dot2(a3.y, qv.y, dot2(a3.x, qv.x, 0.f))));
      slot = slot + 1 == NSLOT ? 0 : slot + 1;
    }
#define TR_STEP(HALF, CTRL, BIT) _Pragma("unroll") for (int hh = 0; hh < (HALF); ++hh) { \
      const float lo_ = xx[hh], hi_ = xx[hh + (HALF)]; const float t1_ = lo_ + dpp<CTRL>(lo_), t2_ = hi_ + dpp<CTRL>(hi_); \
      xx[hh] = (n & (BIT)) ? t2_ : t1_; }
    TR_STEP(8, XOR8, 8) TR_STEP(4, XOR7, 4) TR_STEP(2, XOR2, 2) TR_STEP(1, XOR1, 1)
#undef TR_STEP
    sc0 = sc1; sc1 = sc2; sc2 = sc3;
    sc3 = rbf(rbf(xx[0]) * 0.08838834764831845f);
  }
  unsigned P0, P1, P2, P3;
  {
    float m = fmaxf(fmaxf(sc0, sc1), fmaxf(sc2, sc3));
    m = x32_max(x16_max(max16(m)));
    if (lane == 0) flags[F_MW + k] = __float_as_uint(m);
    lds_arrive((LAS unsigned*)(L + FLAGS) + F_CM, lane);
    lds_wait_gt(flags + F_CM, 3);
    const v4u mw = *(volatile LAS v4u*)(L + FLAGS + F_MW * 4);
    const float M = fmaxf(fmaxf(__uint_as_float(mw.x), __uint_as_float(mw.y)), fmaxf(__uint_as_float(mw.z), __uint_as_float(mw.w)));
    const float p0 = __builtin_amdgcn_exp2f((sc0 - M) * LOG2E), p1 = __builtin_amdgcn_exp2f((sc1 - M) * LOG2E);
    const float p2 = __builtin_amdgcn_exp2f((sc2 - M) * LOG2E), p3 = __builtin_amdgcn_exp2f((sc3 - M) * LOG2E);
    const float lsum = x32_sum(x16_sum(sum16((p0 + p1) + (p2 + p3))));
    auto pk = [](float v) { const unsigned hi = f2bf(v); const unsigned lo = f2bf(v - bf2f(hi)); return hi | (lo << 16); };
    P0 = pk(p0); P1 = pk(p1); P2 = pk(p2); P3 = pk(p3);
    if (lane == 0) flags[F_LW + k] = __float_as_uint(lsum);
    if (lds_arrive((LAS unsigned*)(L + FLAGS) + F_CL, lane) == 3) {
      const v4u lw = *(volatile LAS v4u*)(L + FLAGS + F_LW * 4);
      const float lt = (__uint_as_float(lw.x) + __uint_as_float(lw.y)) + (__uint_as_float(lw.z) + __uint_as_float(lw.w));
      if (lane < 2) gst(p.ws, WS_ML + (cu * 2 + lane) * 8, enc(lane ? lt : M, tag));
    }
  }
  unsigned ob = WS_OP + (cu * 128 + 32 * r + 16 * (n >> 3) + (n & 7)) * 8;
  unsigned oo = WS_OP + ((g * 8 + (n >> 3) * 4) * 128 + 32 * k + 8 * r + (n & 7)) * 8;
  unsigned so = WS_FS + (((j * 18 + (n >> 3) * 9 + 4 * r) * 32 + g) * 8 + (n & 7)) * 8;
  unsigned fo2 = WS_FS + (((j * 18 + min(g, 17)) * 32 + (lane >> 3)) * 8 + (lane & 7)) * 8;
  asm volatile("" : "+v"(ob), "+v"(oo), "+v"(so), "+v"(fo2));
  f32x4 oacc = {0.f, 0.f, 0.f, 0.f};
#pragma unroll 1
  for (int grp = 0; grp < 4; ++grp) {
    const unsigned P = P0; P0 = P1; P1 = P2; P2 = P3;
    static_for<0, 4>([&](auto slc) {
      constexpr int sl = decltype(slc)::value;
      const unsigned v = grp * 4 + sl;
      lds_wait_gt(flags + F_FULL, IT_V + v);
      const LAS v4u* src = (const LAS v4u*)(ringw + slot * SLOT);
      v4u a0 = src[0], a1 = src[64], a2 = src[128], a3 = src[192];
      asm volatile("" : "+v"(a0), "+v"(a1), "+v"(a2), "+v"(a3) :: "memory");
      flags[F_FREE + k] = 16 + v + 1;
      asm volatile("" : "+v"(a0), "+v"(a1), "+v"(a2), "+v"(a3) :: "memory");
      oacc = mfma(a0, make_b(rowbcast<sl * 4 + 0>(P), sel), oacc);
      oacc = mfma(a1, make_b(rowbcast<sl * 4 + 1>(P), sel), oacc);
      oacc = mfma(a2, make_b(rowbcast<sl * 4 + 2>(P), sel), oacc);
      oacc = mfma(a3, make_b(rowbcast<sl * 4 + 3>(P), sel), oacc);
      slot = slot + 1 == NSLOT ? 0 : slot + 1;
    });
  }
  {
#pragma unroll
    for (int i = 0; i < 4; ++i) oacc[i] += dpp<XOR8>(oacc[i]);
    *(LAS f32x4*)(L + TILE_A + k * 1024 + lane * 16) = oacc;
    if (lds_arrive((LAS unsigned*)(L + FLAGS) + F_CO, lane) == 3) {
      f32x4 t = *(LAS f32x4*)(L + TILE_A + lane * 16);
#pragma unroll
      for (int w = 1; w < 4; ++w) t += *(LAS f32x4*)(L + TILE_A + w * 1024 + lane * 16);
      const int hi = n >> 3;
      gst(p.ws, ob, enc(hi ? t[2] : t[0], tag));
      gst(p.ws, ob + 64, enc(hi ? t[3] : t[1], tag));
      asm volatile("" ::: "memory");
      flags[F_OP] = 1u;
    }
  }
  f32x4 sacc;
  {
    lds_wait_gt(flags + F_WR, 0);
    const f32x4 wgt = *(const LAS f32x4*)(L + WGT + (n >> 3) * 16);
    lds_wait_gt(flags + F_FULL, IT_WO);
    v4u wa[8];
    {
      const LAS unsigned char* wb = L + WO_OFF + (32 * k + 8 * r) * 144 + min(n, 8) * 16;
#pragma unroll
      for (int u = 0; u < 8; ++u) wa[u] = *(const LAS v4u*)(wb + u * 144);
    }
    u64 go[4];
    gather4<128 * 8>(p.ws, oo, tag, go);
    float os = wgt[0] * dec(go[0]);
#pragma unroll
    for (int s = 1; s < 4; ++s) os = fmaf(wgt[s], dec(go[s]), os);
    const unsigned o16 = f2bf(os), oth = dppu<XOR8>(o16);
    const unsigned x2 = (n & 8) ? (oth | (o16 << 16)) : (o16 | (oth << 16));
    v4u bo[8];
    static_for<0, 8>([&](auto uc) { constexpr int u = decltype(uc)::value; bo[u] = make_b(rowbcast<u>(x2), sel); });
    __builtin_amdgcn_sched_barrier(0);
    f32x4 s0 = {0.f, 0.f, 0.f, 0.f}, s1 = {0.f, 0.f, 0.f, 0.f};
#pragma unroll
    for (int u = 0; u < 4; ++u) { s0 = mfma(wa[2 * u], bo[2 * u], s0); s1 = mfma(wa[2 * u + 1], bo[2 * u + 1], s1); }
    sacc = s0 + s1;
  }
  *(LAS f32x4*)(L + TILE_B + k * 1024 + lane * 16) = sacc;
  if (lds_arrive((LAS unsigned*)(L + FLAGS) + F_CS, lane) != 3) return;
  {
    f32x4 t = *(LAS f32x4*)(L + TILE_B + lane * 16);
#pragma unroll
    for (int w = 1; w < 4; ++w) t += *(LAS f32x4*)(L + TILE_B + w * 1024 + lane * 16);
    if (lane < 48) gst(p.ws, so, enc(t[0], tag));
    if (lane < 32) {
#pragma unroll
      for (int i = 1; i < 4; ++i) gst(p.ws, so + i * 2048, enc(t[i], tag));
    }
  }
  if (g >= 18) return;
  {
    lds_wait_gt(flags + F_FR, 0);
    const float gate = *(const LAS float*)(L + FSUM + (lane & 7) * 4);
    u64 gs[4];
    gather4<8 * 8 * 8>(p.ws, fo2, tag, gs);
    float t = (dec(gs[0]) + dec(gs[1])) + (dec(gs[2]) + dec(gs[3]));
    t += dpp<XOR8>(t);
    t = x32_sum(x16_sum(t));
    const unsigned o16 = f2bf(bf2f(xres) + rbf(rbf(t) * gate));
    if (lane < 8) ((GAS u16*)p.out)[rb * D + ocol] = (u16)o16;
    if (bid == 0 && lane == 0) gst(p.ws, WS_E, Eu + 1);
  }
}

}

extern "C" void kernel_launch(void* const* d_in, const int* in_sizes, int n_in, void* d_out, int out_size, void* d_ws, size_t ws_size, hipStream_t stream) {
  Params p;
  p.x = (const u16*)d_in[0]; p.kc = (const u16*)d_in[1]; p.vc = (const u16*)d_in[2]; p.wq = (const u16*)d_in[3]; p.wo = (const u16*)d_in[4];
  p.w1 = (const u16*)d_in[6]; p.b1 = (const u16*)d_in[7]; p.w2 = (const u16*)d_in[8]; p.b2 = (const u16*)d_in[9];
  p.out = (u16*)d_out; p.ws = (unsigned char*)d_ws;
  (void)hipFuncSetAttribute((const void*)fused_decode_kernel, hipFuncAttributeMaxDynamicSharedMemorySize, LDS_BYTES);
  hipLaunchKernelGGL(fused_decode_kernel, dim3(NWG), dim3(NTHR), LDS_BYTES, stream, p);
}
```
